```python
import torch

def _softmax(x, axis=-1):
    return torch.softmax(x, dim=axis)
def _gelu(x):
    return torch.nn.functional.gelu(x, approximate="tanh")
def _silu(x):
    return torch.nn.functional.silu(x)
def _sigmoid(x):
    return torch.sigmoid(x)
def _layernorm(x, axis=-1, eps=1e-5):
    m = x.mean(dim=axis, keepdim=True)
    v = x.var(dim=axis, keepdim=True, unbiased=False)
    return (x - m) / torch.sqrt(v + eps)
def _scatter_add(tgt, idx, src, axis=0):
    return tgt.clone().index_add_(axis, idx, src)
def _segment_sum(src, seg, n):
    out = torch.zeros((n,) + src.shape[1:], dtype=src.dtype)
    return out.index_add_(0, seg, src)
def _pad(x, pw):
    flat = tuple(v for pair in reversed(pw) for v in pair)
    return torch.nn.functional.pad(x, flat)

kernel_name = 'recurrent_scan__spectral_gfnet__dense_activation_silu__prompt'

def setup_inputs():

    _w = (lambda a: (lambda t: t.to(torch.bfloat16) if t.is_floating_point() else t)(torch.as_tensor(a)))
    def _A_setup():
        B, S, D = 1, 2048, 512
        Df = D // 2 + 1
        return {
            "x": _w((lambda *s: torch.randn(*s, dtype=torch.bfloat16) * 0.05)(B, S, D)),
            "w_r": _w((lambda *s: torch.randn(*s, dtype=torch.bfloat16) * 3.2)(S, Df) * 0.02),
            "w_i": _w((lambda *s: torch.randn(*s, dtype=torch.bfloat16) * 3.2)(S, Df) * 0.02),
        }
    _A_in = _A_setup()

    def _B_setup():
        B, S, D, F = 1, 2048, 512, 1376
        return {
            "x": _w((lambda *s: torch.randn(*s, dtype=torch.bfloat16) * 0.05)(B, S, D)),
            "w1": _w((lambda *s: torch.randn(*s, dtype=torch.bfloat16) * 3.2)(D, F) * (D ** -0.5)),
            "w2": _w((lambda *s: torch.randn(*s, dtype=torch.bfloat16) * 0.4)(F, D) * (F ** -0.5)),
        }
    _B_in = _B_setup()

    return {
        "A_x": _A_in["x"],
        "A_w_r": _A_in["w_r"],
        "A_w_i": _A_in["w_i"],
        "B_x": _B_in["x"],
        "B_w1": _B_in["w1"],
        "B_w2": _B_in["w2"],
    }

def reference(A_x, A_w_r, A_w_i, B_x, B_w1, B_w2):
    def _A_ref(x, w_r, w_i):
        B, S, D = x.shape
        X = (lambda x, axes=(-2,-1): torch.fft.rfft2(x.float(), dim=axes))(x, axes=(-2, -1))
        W = w_r + 1j * w_i
        X = X * W
        y = (lambda x, s, axes=(-2,-1): torch.fft.irfft2(x, s=s, dim=axes).to(torch.bfloat16))(X, (S, D), axes=(-2, -1))
        return y

    def _B_ref(x, w1, w2):
        h = torch.einsum("bsd,df->bsf", x, w1)
        h = _silu(h)
        out = torch.einsum("bsf,fd->bsd", h, w2)
        return out

    x = A_x
    xn = _layernorm(x, axis=-1)
    s = _A_ref(xn, A_w_r, A_w_i)
    g = _silu(_B_ref(xn, B_w1, B_w2))
    return x + s * g
```

```cpp
#include <hip/hip_runtime.h>
#include <hip/hip_bf16.h>
#include <stdint.h>

#define S_LEN 2048
#define D_LEN 512
#define DF 257
#define F_LEN 1376

typedef unsigned short u16;
using bf16x8 = __attribute__((ext_vector_type(8))) short;
using f32x4 = __attribute__((ext_vector_type(4))) float;

__device__ __forceinline__ float bf2f(u16 u) { return __uint_as_float(((unsigned)u) << 16); }
__device__ __forceinline__ u16 f2bf(float f) {
    __hip_bfloat16 b = __float2bfloat16(f);
    return *reinterpret_cast<u16*>(&b);
}
__device__ __forceinline__ float rbf(float f) { return bf2f(f2bf(f)); }
__device__ __forceinline__ float silu_f(float x) { return x / (1.0f + __expf(-x)); }

__device__ __forceinline__ float2 cmul(float2 a, float2 b) {
    return make_float2(a.x * b.x - a.y * b.y, a.x * b.y + a.y * b.x);
}
__device__ __forceinline__ float2 cadd(float2 a, float2 b) { return make_float2(a.x + b.x, a.y + b.y); }
__device__ __forceinline__ float2 csub(float2 a, float2 b) { return make_float2(a.x - b.x, a.y - b.y); }

template <int SGN>
__device__ __forceinline__ void fft4(float2& a, float2& b, float2& c, float2& d) {
    float2 t0 = cadd(a, c), t1 = csub(a, c), t2 = cadd(b, d), t3 = csub(b, d);
    float2 t3r = (SGN < 0) ? make_float2(t3.y, -t3.x) : make_float2(-t3.y, t3.x);
    a = cadd(t0, t2);
    c = csub(t0, t2);
    b = cadd(t1, t3r);
    d = csub(t1, t3r);
}

template <int SGN>
__device__ __forceinline__ void fft8(float2* v) {
    const float r = 0.70710678118654752f;
    float2 a[8];
#pragma unroll
    for (int i = 0; i < 4; ++i) { a[i] = cadd(v[i], v[i + 4]); a[i + 4] = csub(v[i], v[i + 4]); }
    {
        float2 t = a[5];
        a[5] = (SGN < 0) ? make_float2(r * (t.x + t.y), r * (t.y - t.x)) : make_float2(r * (t.x - t.y), r * (t.x + t.y));
        t = a[6];
        a[6] = (SGN < 0) ? make_float2(t.y, -t.x) : make_float2(-t.y, t.x);
        t = a[7];
        a[7] = (SGN < 0) ? make_float2(r * (t.y - t.x), -r * (t.x + t.y)) : make_float2(-r * (t.x + t.y), r * (t.x - t.y));
    }
    fft4<SGN>(a[0], a[1], a[2], a[3]);
    fft4<SGN>(a[4], a[5], a[6], a[7]);
    v[0] = a[0]; v[2] = a[1]; v[4] = a[2]; v[6] = a[3];
    v[1] = a[4]; v[3] = a[5]; v[5] = a[6]; v[7] = a[7];
}

#define K1_ROWBLK 128
#define K1_W1BLK (8 * 22)
#define K1_W2BLK (22 * 8)
#define K1_WBLK (32 * 5)

__device__ void transpose_tile16(const u16* __restrict__ in, int R, int C, u16* __restrict__ out, int r0, int c0,
                                 u16 (*tile)[65]) {
    int t = threadIdx.x;
    for (int i = t; i < 64 * 64; i += 256) {
        int r = i >> 6, c = i & 63;
        int gr = r0 + r, gc = c0 + c;
        tile[r][c] = (gr < R && gc < C) ? in[(size_t)gr * C + gc] : (u16)0;
    }
    __syncthreads();
    for (int i = t; i < 64 * 64; i += 256) {
        int c = i >> 6, r = i & 63;
        int gr = r0 + r, gc = c0 + c;
        if (gr < R && gc < C) out[(size_t)gc * R + gr] = tile[r][c];
    }
}

__global__ __launch_bounds__(256) void k1_ln_rowfft(const u16* __restrict__ x, const u16* __restrict__ w_r,
                                                     const u16* __restrict__ w_i, const u16* __restrict__ w1,
                                                     const u16* __restrict__ w2, u16* __restrict__ xn,
                                                     float2* __restrict__ XT, u16* __restrict__ w1T,
                                                     u16* __restrict__ w2T, uint32_t* __restrict__ WT) {
    __shared__ float2 smem_tw[512];
    __shared__ float2 smem_buf[4][2][256];
    __shared__ float2 smem_out[256][17];
    int bid = blockIdx.x;
    int t = threadIdx.x;
    if (bid >= K1_ROWBLK) {
        u16(*tile)[65] = reinterpret_cast<u16(*)[65]>(&smem_out[0][0]);
        int b = bid - K1_ROWBLK;
        if (b < K1_W1BLK) {
            transpose_tile16(w1, 512, 1376, w1T, (b / 22) * 64, (b % 22) * 64, tile);
            return;
        }
        b -= K1_W1BLK;
        if (b < K1_W2BLK) {
            transpose_tile16(w2, 1376, 512, w2T, (b / 8) * 64, (b % 8) * 64, tile);
            return;
        }
        b -= K1_W2BLK;
        {
            int s0 = (b / 5) * 64, f0 = (b % 5) * 64;
            uint32_t(*tl)[65] = reinterpret_cast<uint32_t(*)[65]>(&smem_out[0][0]);
            for (int i = t; i < 64 * 64; i += 256) {
                int r = i >> 6, c = i & 63;
                int f = f0 + c;
                uint32_t v = 0;
                if (f < DF) v = (uint32_t)w_r[(size_t)(s0 + r) * DF + f] | ((uint32_t)w_i[(size_t)(s0 + r) * DF + f] << 16);
                tl[r][c] = v;
            }
            __syncthreads();
            for (int i = t; i < 64 * 64; i += 256) {
                int c = i >> 6, r = i & 63;
                int f = f0 + c;
                if (f < DF) WT[(size_t)f * S_LEN + s0 + r] = tl[r][c];
            }
            return;
        }
    }
    for (int k = t; k < 512; k += 256) {
        float sn, cs;
        sincospif(-(float)k / 256.0f, &sn, &cs);
        smem_tw[k] = make_float2(cs, sn);
    }
    __syncthreads();
    int wave = t >> 6, lane = t & 63;
    int s0 = bid * 16;
    for (int rr = 0; rr < 4; ++rr) {
        int rl = wave * 4 + rr;
        int row = s0 + rl;
        const uint32_t* xr = reinterpret_cast<const uint32_t*>(x + (size_t)row * D_LEN);
        float2 v[4];
        float sum = 0.f;
#pragma unroll
        for (int m = 0; m < 4; ++m) {
            uint32_t p = xr[lane + 64 * m];
            v[m] = make_float2(bf2f((u16)(p & 0xffff)), bf2f((u16)(p >> 16)));
            sum += v[m].x + v[m].y;
        }
#pragma unroll
        for (int o = 32; o >= 1; o >>= 1) sum += __shfl_xor(sum, o);
        float mean = sum * (1.0f / 512.0f);
        float sq = 0.f;
#pragma unroll
        for (int m = 0; m < 4; ++m) {
            float a = v[m].x - mean, b = v[m].y - mean;
            sq += a * a + b * b;
        }
#pragma unroll
        for (int o = 32; o >= 1; o >>= 1) sq += __shfl_xor(sq, o);
        float var = sq * (1.0f / 512.0f);
        float mean_b = rbf(mean), var_b = rbf(var);
        float den = rbf(sqrtf(rbf(var_b + 1e-5f)));
        uint32_t* xnr = reinterpret_cast<uint32_t*>(xn + (size_t)row * D_LEN);
#pragma unroll
        for (int m = 0; m < 4; ++m) {
            u16 a = f2bf(rbf(v[m].x - mean_b) / den);
            u16 b = f2bf(rbf(v[m].y - mean_b) / den);
            xnr[lane + 64 * m] = (uint32_t)a | ((uint32_t)b << 16);
            v[m] = make_float2(bf2f(a), bf2f(b));
        }
        float2* b0 = smem_buf[wave][0];
        float2* b1 = smem_buf[wave][1];
        int j = lane;
        fft4<-1>(v[0], v[1], v[2], v[3]);
#pragma unroll
        for (int r = 0; r < 4; ++r) b0[j * 4 + r] = v[r];
        __builtin_amdgcn_wave_barrier();
        __builtin_amdgcn_fence(__ATOMIC_SEQ_CST, "wavefront");
#pragma unroll
        for (int r = 0; r < 4; ++r) v[r] = b0[j + r * 64];
#pragma unroll
        for (int r = 1; r < 4; ++r) v[r] = cmul(v[r], smem_tw[r * (j & 3) * 32]);
        fft4<-1>(v[0], v[1], v[2], v[3]);
#pragma unroll
        for (int r = 0; r < 4; ++r) b1[(j >> 2) * 16 + (j & 3) + r * 4] = v[r];
        __builtin_amdgcn_wave_barrier();
        __builtin_amdgcn_fence(__ATOMIC_SEQ_CST, "wavefront");
#pragma unroll
        for (int r = 0; r < 4; ++r) v[r] = b1[j + r * 64];
#pragma unroll
        for (int r = 1; r < 4; ++r) v[r] = cmul(v[r], smem_tw[r * (j & 15) * 8]);
        fft4<-1>(v[0], v[1], v[2], v[3]);
#pragma unroll
        for (int r = 0; r < 4; ++r) b0[(j >> 4) * 64 + (j & 15) + r * 16] = v[r];
        __builtin_amdgcn_wave_barrier();
        __builtin_amdgcn_fence(__ATOMIC_SEQ_CST, "wavefront");
#pragma unroll
        for (int r = 0; r < 4; ++r) v[r] = b0[j + r * 64];
#pragma unroll
        for (int r = 1; r < 4; ++r) v[r] = cmul(v[r], smem_tw[r * j * 2]);
        fft4<-1>(v[0], v[1], v[2], v[3]);
#pragma unroll
        for (int r = 0; r < 4; ++r) b1[j + r * 64] = v[r];
        __builtin_amdgcn_wave_barrier();
        __builtin_amdgcn_fence(__ATOMIC_SEQ_CST, "wavefront");
#pragma unroll
        for (int r = 0; r < 4; ++r) {
            int k = j + r * 64;
            float2 zk = b1[k];
            float2 zm = b1[(256 - k) & 255];
            float2 e = make_float2(0.5f * (zk.x + zm.x), 0.5f * (zk.y - zm.y));
            float2 o = make_float2(0.5f * (zk.x - zm.x), 0.5f * (zk.y + zm.y));
            float2 wo = cmul(smem_tw[k], o);
            float2 X = make_float2(e.x + wo.y, e.y - wo.x);
            if (k == 0) X = make_float2(zk.x + zk.y, zk.x - zk.y);
            smem_out[k][rl] = X;
        }
        __builtin_amdgcn_wave_barrier();
        __builtin_amdgcn_fence(__ATOMIC_SEQ_CST, "wavefront");
    }
    __syncthreads();
    for (int i = t; i < 256 * 16; i += 256) {
        int f = i >> 4, r = i & 15;
        XT[(size_t)f * S_LEN + s0 + r] = smem_out[f][r];
    }
}

template <int SGN>
__device__ __forceinline__ void fft2048(float2* A, float2* B, const float2* tw, int t) {
    float2 v[8];
    const int j = t;
#pragma unroll
    for (int r = 0; r < 8; ++r) v[r] = A[j + r * 256];
    fft8<SGN>(v);
#pragma unroll
    for (int r = 0; r < 8; ++r) B[j * 8 + r] = v[r];
    __syncthreads();
#pragma unroll
    for (int r = 0; r < 8; ++r) v[r] = B[j + r * 256];
#pragma unroll
    for (int r = 1; r < 8; ++r) {
        float2 w = tw[r * (j & 7) * 32];
        if (SGN > 0) w.y = -w.y;
        v[r] = cmul(v[r], w);
    }
    fft8<SGN>(v);
#pragma unroll
    for (int r = 0; r < 8; ++r) A[(j >> 3) * 64 + (j & 7) + r * 8] = v[r];
    __syncthreads();
#pragma unroll
    for (int r = 0; r < 8; ++r) v[r] = A[j + r * 256];
#pragma unroll
    for (int r = 1; r < 8; ++r) {
        float2 w = tw[r * (j & 63) * 4];
        if (SGN > 0) w.y = -w.y;
        v[r] = cmul(v[r], w);
    }
    fft8<SGN>(v);
#pragma unroll
    for (int r = 0; r < 8; ++r) B[(j >> 6) * 512 + (j & 63) + r * 64] = v[r];
    __syncthreads();
#pragma unroll
    for (int h = 0; h < 2; ++h) {
        int jj = j + h * 256;
        float2 u[4];
#pragma unroll
        for (int r = 0; r < 4; ++r) u[r] = B[jj + r * 512];
#pragma unroll
        for (int r = 1; r < 4; ++r) {
            float2 w = tw[r * jj];
            if (SGN > 0) w.y = -w.y;
            u[r] = cmul(u[r], w);
        }
        fft4<SGN>(u[0], u[1], u[2], u[3]);
#pragma unroll
        for (int r = 0; r < 4; ++r) A[jj + r * 512] = u[r];
    }
    __syncthreads();
}

__global__ __launch_bounds__(256) void k2_colfft(float2* __restrict__ XT, const uint32_t* __restrict__ WT) {
    __shared__ float2 A[2048];
    __shared__ float2 B[2048];
    __shared__ float2 tw[2048];
    int t = threadIdx.x;
    int f = blockIdx.x;
    for (int k = t; k < 2048; k += 256) {
        float sn, cs;
        sincospif(-(float)k / 1024.0f, &sn, &cs);
        tw[k] = make_float2(cs, sn);
    }
    float2* col = XT + (size_t)f * S_LEN;
    for (int k = t; k < 2048; k += 256) A[k] = col[k];
    __syncthreads();
    fft2048<-1>(A, B, tw, t);
    if (f != 0) {
        const uint32_t* wc = WT + (size_t)f * S_LEN;
        for (int k = t; k < 2048; k += 256) {
            uint32_t p = wc[k];
            float2 w = make_float2(bf2f((u16)(p & 0xffff)), bf2f((u16)(p >> 16)));
            A[k] = cmul(A[k], w);
        }
        __syncthreads();
    } else {
        const uint32_t* w0 = WT;
        const uint32_t* wn = WT + (size_t)256 * S_LEN;
        float2 res[8];
#pragma unroll
        for (int i = 0; i < 8; ++i) {
            int k = t + i * 256;
            int km = (2048 - k) & 2047;
            float2 zk = A[k], zm = A[km];
            float2 f0k = make_float2(0.5f * (zk.x + zm.x), 0.5f * (zk.y - zm.y));
            float2 fnk = make_float2(0.5f * (zk.y + zm.y), -0.5f * (zk.x - zm.x));
            float2 f0m = make_float2(f0k.x, -f0k.y);
            float2 fnm = make_float2(fnk.x, -fnk.y);
            uint32_t p;
            p = w0[k];  float2 w0k = make_float2(bf2f((u16)(p & 0xffff)), bf2f((u16)(p >> 16)));
            p = w0[km]; float2 w0m = make_float2(bf2f((u16)(p & 0xffff)), bf2f((u16)(p >> 16)));
            p = wn[k];  float2 wnk = make_float2(bf2f((u16)(p & 0xffff)), bf2f((u16)(p >> 16)));
            p = wn[km]; float2 wnm = make_float2(bf2f((u16)(p & 0xffff)), bf2f((u16)(p >> 16)));
            float2 y0k = cmul(f0k, w0k), y0m = cmul(f0m, w0m);
            float2 ynk = cmul(fnk, wnk), ynm = cmul(fnm, wnm);
            float2 h0 = make_float2(0.5f * (y0k.x + y0m.x), 0.5f * (y0k.y - y0m.y));
            float2 hn = make_float2(0.5f * (ynk.x + ynm.x), 0.5f * (ynk.y - ynm.y));
            res[i] = make_float2(h0.x - hn.y, h0.y + hn.x);
        }
        __syncthreads();
#pragma unroll
        for (int i = 0; i < 8; ++i) A[t + i * 256] = res[i];
        __syncthreads();
    }
    fft2048<1>(A, B, tw, t);
    for (int k = t; k < 2048; k += 256) col[k] = A[k];
}

template <int K, int N>
__global__ __launch_bounds__(256) void gemm_nt_silu(const u16* __restrict__ A, const u16* __restrict__ Bt,
                                                     u16* __restrict__ C) {
    int wave = threadIdx.x >> 6, lane = threadIdx.x & 63;
    int nb = (N + 63) / 64;
    int bm = blockIdx.x / nb, bn = blockIdx.x % nb;
    int m0 = bm * 128 + (wave >> 1) * 64;
    int n0 = bn * 64 + (wave & 1) * 32;
    if (n0 >= N) return;
    f32x4 acc[4][2];
#pragma unroll
    for (int i = 0; i < 4; ++i)
#pragma unroll
        for (int j = 0; j < 2; ++j) acc[i][j] = (f32x4){0.f, 0.f, 0.f, 0.f};
    const u16* ap = A + (size_t)(m0 + (lane & 15)) * K + 8 * (lane >> 4);
    const u16* bp = Bt + (size_t)(n0 + (lane & 15)) * K + 8 * (lane >> 4);
    for (int k0 = 0; k0 < K; k0 += 32) {
        bf16x8 af[4], bfr[2];
#pragma unroll
        for (int i = 0; i < 4; ++i) af[i] = *reinterpret_cast<const bf16x8*>(ap + (size_t)i * 16 * K + k0);
#pragma unroll
        for (int j = 0; j < 2; ++j) bfr[j] = *reinterpret_cast<const bf16x8*>(bp + (size_t)j * 16 * K + k0);
#pragma unroll
        for (int i = 0; i < 4; ++i)
#pragma unroll
            for (int j = 0; j < 2; ++j)
                acc[i][j] = __builtin_amdgcn_mfma_f32_16x16x32_bf16(bfr[j], af[i], acc[i][j], 0, 0, 0);
    }
#pragma unroll
    for (int i = 0; i < 4; ++i)
#pragma unroll
        for (int j = 0; j < 2; ++j) {
            int m = m0 + i * 16 + (lane & 15);
            int n = n0 + j * 16 + (lane >> 4) * 4;
            u16 o[4];
#pragma unroll
            for (int r = 0; r < 4; ++r) o[r] = f2bf(silu_f(rbf(acc[i][j][r])));
            uint2 pk = make_uint2((uint32_t)o[0] | ((uint32_t)o[1] << 16), (uint32_t)o[2] | ((uint32_t)o[3] << 16));
            *reinterpret_cast<uint2*>(C + (size_t)m * N + n) = pk;
        }
}

__global__ __launch_bounds__(256) void k3_rowc2r(const float2* __restrict__ XT, const u16* __restrict__ x,
                                                  const u16* __restrict__ g, u16* __restrict__ out) {
    __shared__ float2 smem_tw[512];
    __shared__ float2 smem_buf[4][2][256];
    __shared__ float2 smem_in[256][17];
    int t = threadIdx.x;
    int s0 = blockIdx.x * 16;
    for (int k = t; k < 512; k += 256) {
        float sn, cs;
        sincospif(-(float)k / 256.0f, &sn, &cs);
        smem_tw[k] = make_float2(cs, sn);
    }
    for (int i = t; i < 256 * 16; i += 256) {
        int f = i >> 4, r = i & 15;
        smem_in[f][r] = XT[(size_t)f * S_LEN + s0 + r];
    }
    __syncthreads();
    int wave = t >> 6, lane = t & 63;
    for (int rr = 0; rr < 4; ++rr) {
        int rl = wave * 4 + rr;
        int row = s0 + rl;
        int j = lane;
        float2 v[4];
#pragma unroll
        for (int r = 0; r < 4; ++r) {
            int k = j + r * 64;
            float2 xk, xm;
            if (k == 0) {
                float2 p = smem_in[0][rl];
                xk = make_float2(p.x, 0.f);
                xm = make_float2(p.y, 0.f);
            } else {
                xk = smem_in[k][rl];
                xm = smem_in[256 - k][rl];
            }
            float2 e = make_float2(xk.x + xm.x, xk.y - xm.y);
            float2 o = make_float2(xk.x - xm.x, xk.y + xm.y);
            float2 w = smem_tw[k];
            w.y = -w.y;
            float2 wo = cmul(w, o);
            v[r] = make_float2(e.x - wo.y, e.y + wo.x);
        }
        float2* b0 = smem_buf[wave][0];
        float2* b1 = smem_buf[wave][1];
        fft4<1>(v[0], v[1], v[2], v[3]);
#pragma unroll
        for (int r = 0; r < 4; ++r) b0[j * 4 + r] = v[r];
        __builtin_amdgcn_wave_barrier();
        __builtin_amdgcn_fence(__ATOMIC_SEQ_CST, "wavefront");
#pragma unroll
        for (int r = 0; r < 4; ++r) v[r] = b0[j + r * 64];
#pragma unroll
        for (int r = 1; r < 4; ++r) { float2 w = smem_tw[r * (j & 3) * 32]; w.y = -w.y; v[r] = cmul(v[r], w); }
        fft4<1>(v[0], v[1], v[2], v[3]);
#pragma unroll
        for (int r = 0; r < 4; ++r) b1[(j >> 2) * 16 + (j & 3) + r * 4] = v[r];
        __builtin_amdgcn_wave_barrier();
        __builtin_amdgcn_fence(__ATOMIC_SEQ_CST, "wavefront");
#pragma unroll
        for (int r = 0; r < 4; ++r) v[r] = b1[j + r * 64];
#pragma unroll
        for (int r = 1; r < 4; ++r) { float2 w = smem_tw[r * (j & 15) * 8]; w.y = -w.y; v[r] = cmul(v[r], w); }
        fft4<1>(v[0], v[1], v[2], v[3]);
#pragma unroll
        for (int r = 0; r < 4; ++r) b0[(j >> 4) * 64 + (j & 15) + r * 16] = v[r];
        __builtin_amdgcn_wave_barrier();
        __builtin_amdgcn_fence(__ATOMIC_SEQ_CST, "wavefront");
#pragma unroll
        for (int r = 0; r < 4; ++r) v[r] = b0[j + r * 64];
#pragma unroll
        for (int r = 1; r < 4; ++r) { float2 w = smem_tw[r * j * 2]; w.y = -w.y; v[r] = cmul(v[r], w); }
        fft4<1>(v[0], v[1], v[2], v[3]);
        const float scale = 1.0f / ((float)S_LEN * (float)D_LEN);
        const uint32_t* xr = reinterpret_cast<const uint32_t*>(x + (size_t)row * D_LEN);
        const uint32_t* gr = reinterpret_cast<const uint32_t*>(g + (size_t)row * D_LEN);
        uint32_t* orow = reinterpret_cast<uint32_t*>(out + (size_t)row * D_LEN);
#pragma unroll
        for (int r = 0; r < 4; ++r) {
            int n = j + r * 64;
            uint32_t xp = xr[n], gp = gr[n];
            float s0v = rbf(v[r].x * scale), s1v = rbf(v[r].y * scale);
            float o0 = bf2f((u16)(xp & 0xffff)) + rbf(s0v * bf2f((u16)(gp & 0xffff)));
            float o1 = bf2f((u16)(xp >> 16)) + rbf(s1v * bf2f((u16)(gp >> 16)));
            orow[n] = (uint32_t)f2bf(o0) | ((uint32_t)f2bf(o1) << 16);
        }
        __builtin_amdgcn_wave_barrier();
        __builtin_amdgcn_fence(__ATOMIC_SEQ_CST, "wavefront");
    }
}

extern "C" void kernel_launch(void* const* d_in, const int* in_sizes, int n_in, void* d_out, int out_size, void* d_ws,
                              size_t ws_size, hipStream_t stream) {
    const u16* x = (const u16*)d_in[0];
    const u16* w_r = (const u16*)d_in[1];
    const u16* w_i = (const u16*)d_in[2];
    const u16* w1 = (const u16*)d_in[4];
    const u16* w2 = (const u16*)d_in[5];
    u16* out = (u16*)d_out;
    char* ws = (char*)d_ws;
    u16* xn = (u16*)(ws);
    float2* XT = (float2*)(ws + (2u << 20));
    u16* w1T = (u16*)(ws + (6u << 20));
    u16* w2T = (u16*)(ws + (8u << 20));
    uint32_t* WT = (uint32_t*)(ws + (10u << 20));
    u16* h = (u16*)(ws + (13u << 20));
    u16* g = (u16*)(ws + (19u << 20));

    k1_ln_rowfft<<<K1_ROWBLK + K1_W1BLK + K1_W2BLK + K1_WBLK, 256, 0, stream>>>(x, w_r, w_i, w1, w2, xn, XT, w1T, w2T, WT);
    gemm_nt_silu<512, 1376><<<16 * 22, 256, 0, stream>>>(xn, w1T, h);
    k2_colfft<<<256, 256, 0, stream>>>(XT, WT);
    gemm_nt_silu<1376, 512><<<16 * 8, 256, 0, stream>>>(h, w2T, g);
    k3_rowc2r<<<128, 256, 0, stream>>>(XT, x, g, out);
}
```

```cpp
#include <hip/hip_runtime.h>
#include <hip/hip_bf16.h>
#include <stdint.h>

#define S_LEN 2048
#define D_LEN 512
#define DF 257
#define F_LEN 1376

typedef unsigned short u16;
using bf16x8 = __attribute__((ext_vector_type(8))) short;
using f32x4 = __attribute__((ext_vector_type(4))) float;

__device__ __forceinline__ float bf2f(u16 u) { return __uint_as_float(((unsigned)u) << 16); }
__device__ __forceinline__ u16 f2bf(float f) {
    __hip_bfloat16 b = __float2bfloat16(f);
    return *reinterpret_cast<u16*>(&b);
}
__device__ __forceinline__ float rbf(float f) { return bf2f(f2bf(f)); }
__device__ __forceinline__ float silu_f(float x) { return x / (1.0f + __expf(-x)); }

__device__ __forceinline__ float2 cmul(float2 a, float2 b) {
    return make_float2(a.x * b.x - a.y * b.y, a.x * b.y + a.y * b.x);
}
__device__ __forceinline__ float2 cadd(float2 a, float2 b) { return make_float2(a.x + b.x, a.y + b.y); }
__device__ __forceinline__ float2 csub(float2 a, float2 b) { return make_float2(a.x - b.x, a.y - b.y); }

template <int SGN>
__device__ __forceinline__ void fft4(float2& a, float2& b, float2& c, float2& d) {
    float2 t0 = cadd(a, c), t1 = csub(a, c), t2 = cadd(b, d), t3 = csub(b, d);
    float2 t3r = (SGN < 0) ? make_float2(t3.y, -t3.x) : make_float2(-t3.y, t3.x);
    a = cadd(t0, t2);
    c = csub(t0, t2);
    b = cadd(t1, t3r);
    d = csub(t1, t3r);
}

template <int SGN>
__device__ __forceinline__ void fft8(float2* v) {
    const float r = 0.70710678118654752f;
    float2 a[8];
#pragma unroll
    for (int i = 0; i < 4; ++i) { a[i] = cadd(v[i], v[i + 4]); a[i + 4] = csub(v[i], v[i + 4]); }
    {
        float2 t = a[5];
        a[5] = (SGN < 0) ? make_float2(r * (t.x + t.y), r * (t.y - t.x)) : make_float2(r * (t.x - t.y), r * (t.x + t.y));
        t = a[6];
        a[6] = (SGN < 0) ? make_float2(t.y, -t.x) : make_float2(-t.y, t.x);
        t = a[7];
        a[7] = (SGN < 0) ? make_float2(r * (t.y - t.x), -r * (t.x + t.y)) : make_float2(-r * (t.x + t.y), r * (t.x - t.y));
    }
    fft4<SGN>(a[0], a[1], a[2], a[3]);
    fft4<SGN>(a[4], a[5], a[6], a[7]);
    v[0] = a[0]; v[2] = a[1]; v[4] = a[2]; v[6] = a[3];
    v[1] = a[4]; v[3] = a[5]; v[5] = a[6]; v[7] = a[7];
}

#define K1_ROWBLK 128
#define K1_W1BLK (8 * 22)
#define K1_W2BLK (22 * 8)
#define K1_WBLK (32 * 5)

__device__ void transpose_tile16(const u16* __restrict__ in, int R, int C, u16* __restrict__ out, int r0, int c0,
                                 u16 (*tile)[65]) {
    int t = threadIdx.x;
    for (int i = t; i < 64 * 64; i += 256) {
        int r = i >> 6, c = i & 63;
        int gr = r0 + r, gc = c0 + c;
        tile[r][c] = (gr < R && gc < C) ? in[(size_t)gr * C + gc] : (u16)0;
    }
    __syncthreads();
    for (int i = t; i < 64 * 64; i += 256) {
        int c = i >> 6, r = i & 63;
        int gr = r0 + r, gc = c0 + c;
        if (gr < R && gc < C) out[(size_t)gc * R + gr] = tile[r][c];
    }
}

__global__ __launch_bounds__(256) void k1_ln_rowfft(const u16* __restrict__ x, const u16* __restrict__ w_r,
                                                     const u16* __restrict__ w_i, const u16* __restrict__ w1,
                                                     const u16* __restrict__ w2, u16* __restrict__ xn,
                                                     float2* __restrict__ XT, u16* __restrict__ w1T,
                                                     u16* __restrict__ w2T, uint32_t* __restrict__ WT) {
    __shared__ float2 smem_tw[512];
    __shared__ float2 smem_buf[4][2][256];
    __shared__ float2 smem_out[256][17];
    int bid = blockIdx.x;
    int t = threadIdx.x;
    if (bid >= K1_ROWBLK) {
        u16(*tile)[65] = reinterpret_cast<u16(*)[65]>(&smem_out[0][0]);
        int b = bid - K1_ROWBLK;
        if (b < K1_W1BLK) {
            transpose_tile16(w1, 512, 1376, w1T, (b / 22) * 64, (b % 22) * 64, tile);
            return;
        }
        b -= K1_W1BLK;
        if (b < K1_W2BLK) {
            transpose_tile16(w2, 1376, 512, w2T, (b / 8) * 64, (b % 8) * 64, tile);
            return;
        }
        b -= K1_W2BLK;
        {
            int s0 = (b / 5) * 64, f0 = (b % 5) * 64;
            uint32_t(*tl)[65] = reinterpret_cast<uint32_t(*)[65]>(&smem_out[0][0]);
            for (int i = t; i < 64 * 64; i += 256) {
                int r = i >> 6, c = i & 63;
                int f = f0 + c;
                uint32_t v = 0;
                if (f < DF) v = (uint32_t)w_r[(size_t)(s0 + r) * DF + f] | ((uint32_t)w_i[(size_t)(s0 + r) * DF + f] << 16);
                tl[r][c] = v;
            }
            __syncthreads();
            for (int i = t; i < 64 * 64; i += 256) {
                int c = i >> 6, r = i & 63;
                int f = f0 + c;
                if (f < DF) WT[(size_t)f * S_LEN + s0 + r] = tl[r][c];
            }
            return;
        }
    }
    for (int k = t; k < 512; k += 256) {
        float sn, cs;
        sincospif(-(float)k / 256.0f, &sn, &cs);
        smem_tw[k] = make_float2(cs, sn);
    }
    __syncthreads();
    int wave = t >> 6, lane = t & 63;
    int s0 = bid * 16;
    for (int rr = 0; rr < 4; ++rr) {
        int rl = wave * 4 + rr;
        int row = s0 + rl;
        const uint32_t* xr = reinterpret_cast<const uint32_t*>(x + (size_t)row * D_LEN);
        float2 v[4];
        float sum = 0.f;
#pragma unroll
        for (int m = 0; m < 4; ++m) {
            uint32_t p = xr[lane + 64 * m];
            v[m] = make_float2(bf2f((u16)(p & 0xffff)), bf2f((u16)(p >> 16)));
            sum += v[m].x + v[m].y;
        }
#pragma unroll
        for (int o = 32; o >= 1; o >>= 1) sum += __shfl_xor(sum, o);
        float mean = sum * (1.0f / 512.0f);
        float sq = 0.f;
#pragma unroll
        for (int m = 0; m < 4; ++m) {
            float a = v[m].x - mean, b = v[m].y - mean;
            sq += a * a + b * b;
        }
#pragma unroll
        for (int o = 32; o >= 1; o >>= 1) sq += __shfl_xor(sq, o);
        float var = sq * (1.0f / 512.0f);
        float mean_b = rbf(mean), var_b = rbf(var);
        float den = rbf(sqrtf(rbf(var_b + 1e-5f)));
        uint32_t* xnr = reinterpret_cast<uint32_t*>(xn + (size_t)row * D_LEN);
#pragma unroll
        for (int m = 0; m < 4; ++m) {
            u16 a = f2bf(rbf(v[m].x - mean_b) / den);
            u16 b = f2bf(rbf(v[m].y - mean_b) / den);
            xnr[lane + 64 * m] = (uint32_t)a | ((uint32_t)b << 16);
            v[m] = make_float2(bf2f(a), bf2f(b));
        }
        float2* b0 = smem_buf[wave][0];
        float2* b1 = smem_buf[wave][1];
        int j = lane;
        fft4<-1>(v[0], v[1], v[2], v[3]);
#pragma unroll
        for (int r = 0; r < 4; ++r) b0[j * 4 + r] = v[r];
        __builtin_amdgcn_wave_barrier();
        __builtin_amdgcn_fence(__ATOMIC_SEQ_CST, "wavefront");
#pragma unroll
        for (int r = 0; r < 4; ++r) v[r] = b0[j + r * 64];
#pragma unroll
        for (int r = 1; r < 4; ++r) v[r] = cmul(v[r], smem_tw[r * (j & 3) * 32]);
        fft4<-1>(v[0], v[1], v[2], v[3]);
#pragma unroll
        for (int r = 0; r < 4; ++r) b1[(j >> 2) * 16 + (j & 3) + r * 4] = v[r];
        __builtin_amdgcn_wave_barrier();
        __builtin_amdgcn_fence(__ATOMIC_SEQ_CST, "wavefront");
#pragma unroll
        for (int r = 0; r < 4; ++r) v[r] = b1[j + r * 64];
#pragma unroll
        for (int r = 1; r < 4; ++r) v[r] = cmul(v[r], smem_tw[r * (j & 15) * 8]);
        fft4<-1>(v[0], v[1], v[2], v[3]);
#pragma unroll
        for (int r = 0; r < 4; ++r) b0[(j >> 4) * 64 + (j & 15) + r * 16] = v[r];
        __builtin_amdgcn_wave_barrier();
        __builtin_amdgcn_fence(__ATOMIC_SEQ_CST, "wavefront");
#pragma unroll
        for (int r = 0; r < 4; ++r) v[r] = b0[j + r * 64];
#pragma unroll
        for (int r = 1; r < 4; ++r) v[r] = cmul(v[r], smem_tw[r * j * 2]);
        fft4<-1>(v[0], v[1], v[2], v[3]);
#pragma unroll
        for (int r = 0; r < 4; ++r) b1[j + r * 64] = v[r];
        __builtin_amdgcn_wave_barrier();
        __builtin_amdgcn_fence(__ATOMIC_SEQ_CST, "wavefront");
#pragma unroll
        for (int r = 0; r < 4; ++r) {
            int k = j + r * 64;
            float2 zk = b1[k];
            float2 zm = b1[(256 - k) & 255];
            float2 e = make_float2(0.5f * (zk.x + zm.x), 0.5f * (zk.y - zm.y));
            float2 o = make_float2(0.5f * (zk.x - zm.x), 0.5f * (zk.y + zm.y));
            float2 wo = cmul(smem_tw[k], o);
            float2 X = make_float2(e.x + wo.y, e.y - wo.x);
            if (k == 0) X = make_float2(zk.x + zk.y, zk.x - zk.y);
            smem_out[k][rl] = X;
        }
        __builtin_amdgcn_wave_barrier();
        __builtin_amdgcn_fence(__ATOMIC_SEQ_CST, "wavefront");
    }
    __syncthreads();
    for (int i = t; i < 256 * 16; i += 256) {
        int f = i >> 4, r = i & 15;
        XT[(size_t)f * S_LEN + s0 + r] = smem_out[f][r];
    }
}

template <int SGN>
__device__ __forceinline__ void fft2048(float2* A, float2* B, const float2* tw, int t) {
    float2 v[8];
    const int j = t;
#pragma unroll
    for (int r = 0; r < 8; ++r) v[r] = A[j + r * 256];
    fft8<SGN>(v);
#pragma unroll
    for (int r = 0; r < 8; ++r) B[j * 8 + r] = v[r];
    __syncthreads();
#pragma unroll
    for (int r = 0; r < 8; ++r) v[r] = B[j + r * 256];
#pragma unroll
    for (int r = 1; r < 8; ++r) {
        float2 w = tw[r * (j & 7) * 32];
        if (SGN > 0) w.y = -w.y;
        v[r] = cmul(v[r], w);
    }
    fft8<SGN>(v);
#pragma unroll
    for (int r = 0; r < 8; ++r) A[(j >> 3) * 64 + (j & 7) + r * 8] = v[r];
    __syncthreads();
#pragma unroll
    for (int r = 0; r < 8; ++r) v[r] = A[j + r * 256];
#pragma unroll
    for (int r = 1; r < 8; ++r) {
        float2 w = tw[r * (j & 63) * 4];
        if (SGN > 0) w.y = -w.y;
        v[r] = cmul(v[r], w);
    }
    fft8<SGN>(v);
#pragma unroll
    for (int r = 0; r < 8; ++r) B[(j >> 6) * 512 + (j & 63) + r * 64] = v[r];
    __syncthreads();
#pragma unroll
    for (int h = 0; h < 2; ++h) {
        int jj = j + h * 256;
        float2 u[4];
#pragma unroll
        for (int r = 0; r < 4; ++r) u[r] = B[jj + r * 512];
#pragma unroll
        for (int r = 1; r < 4; ++r) {
            float2 w = tw[r * jj];
            if (SGN > 0) w.y = -w.y;
            u[r] = cmul(u[r], w);
        }
        fft4<SGN>(u[0], u[1], u[2], u[3]);
#pragma unroll
        for (int r = 0; r < 4; ++r) A[jj + r * 512] = u[r];
    }
    __syncthreads();
}

__global__ __launch_bounds__(256) void k2_colfft(float2* __restrict__ XT, const uint32_t* __restrict__ WT) {
    __shared__ float2 A[2048];
    __shared__ float2 B[2048];
    __shared__ float2 tw[2048];
    int t = threadIdx.x;
    int f = blockIdx.x;
    for (int k = t; k < 2048; k += 256) {
        float sn, cs;
        sincospif(-(float)k / 1024.0f, &sn, &cs);
        tw[k] = make_float2(cs, sn);
    }
    float2* col = XT + (size_t)f * S_LEN;
    for (int k = t; k < 2048; k += 256) A[k] = col[k];
    __syncthreads();
    fft2048<-1>(A, B, tw, t);
    if (f != 0) {
        const uint32_t* wc = WT + (size_t)f * S_LEN;
        for (int k = t; k < 2048; k += 256) {
            uint32_t p = wc[k];
            float2 w = make_float2(bf2f((u16)(p & 0xffff)), bf2f((u16)(p >> 16)));
            A[k] = cmul(A[k], w);
        }
        __syncthreads();
    } else {
        const uint32_t* w0 = WT;
        const uint32_t* wn = WT + (size_t)256 * S_LEN;
        float2 res[8];
#pragma unroll
        for (int i = 0; i < 8; ++i) {
            int k = t + i * 256;
            int km = (2048 - k) & 2047;
            float2 zk = A[k], zm = A[km];
            float2 f0k = make_float2(0.5f * (zk.x + zm.x), 0.5f * (zk.y - zm.y));
            float2 fnk = make_float2(0.5f * (zk.y + zm.y), -0.5f * (zk.x - zm.x));
            float2 f0m = make_float2(f0k.x, -f0k.y);
            float2 fnm = make_float2(fnk.x, -fnk.y);
            uint32_t p;
            p = w0[k];  float2 w0k = make_float2(bf2f((u16)(p & 0xffff)), bf2f((u16)(p >> 16)));
            p = w0[km]; float2 w0m = make_float2(bf2f((u16)(p & 0xffff)), bf2f((u16)(p >> 16)));
            p = wn[k];  float2 wnk = make_float2(bf2f((u16)(p & 0xffff)), bf2f((u16)(p >> 16)));
            p = wn[km]; float2 wnm = make_float2(bf2f((u16)(p & 0xffff)), bf2f((u16)(p >> 16)));
            float2 y0k = cmul(f0k, w0k), y0m = cmul(f0m, w0m);
            float2 ynk = cmul(fnk, wnk), ynm = cmul(fnm, wnm);
            float2 h0 = make_float2(0.5f * (y0k.x + y0m.x), 0.5f * (y0k.y - y0m.y));
            float2 hn = make_float2(0.5f * (ynk.x + ynm.x), 0.5f * (ynk.y - ynm.y));
            res[i] = make_float2(h0.x - hn.y, h0.y + hn.x);
        }
        __syncthreads();
#pragma unroll
        for (int i = 0; i < 8; ++i) A[t + i * 256] = res[i];
        __syncthreads();
    }
    fft2048<1>(A, B, tw, t);
    for (int k = t; k < 2048; k += 256) col[k] = A[k];
}

template <int K, int N>
__device__ __forceinline__ void gemm_tile_silu(const u16* __restrict__ A, const u16* __restrict__ Bt,
                                               u16* __restrict__ C, int bid, char* ldsbase) {
    constexpr int BM = 64, BN = 64, BK = 64, PF = 3;
    constexpr int NST = (K + BK - 1) / BK;
    constexpr int ACH = 2, BCH = 2;
    constexpr int STG = (BM + BN) * 128;
    int t = threadIdx.x, wave = t >> 6, lane = t & 63;
    constexpr int nb = (N + BN - 1) / BN;
    int bm = bid / nb, bn = bid % nb;
    int m0 = bm * BM, n0 = bn * BN;
    int wr = wave >> 1, wc = wave & 1;
    const u16* ag[ACH];
    const u16* bg[BCH];
    int aoff[ACH], boff[BCH];
#pragma unroll
    for (int i = 0; i < ACH; ++i) {
        int c = t + i * 256, r = c >> 3, ch = c & 7;
        ag[i] = A + (size_t)(m0 + r) * K + ch * 8;
        aoff[i] = r * 128 + ((ch ^ (r & 7)) << 4);
    }
#pragma unroll
    for (int i = 0; i < BCH; ++i) {
        int c = t + i * 256, r = c >> 3, ch = c & 7;
        int gr = n0 + r;
        gr = gr < N ? gr : N - 1;
        bg[i] = Bt + (size_t)gr * K + ch * 8;
        boff[i] = (BM + r) * 128 + ((ch ^ (r & 7)) << 4);
    }
    uint4 ra[PF][ACH], rb[PF][BCH];
    auto gload = [&](int slot, int s) {
#pragma unroll
        for (int i = 0; i < ACH; ++i) {
            int ch = (t + i * 256) & 7;
            bool ok = (s * BK + ch * 8) < K;
            ra[slot][i] = ok ? *reinterpret_cast<const uint4*>(ag[i] + s * BK) : make_uint4(0, 0, 0, 0);
        }
#pragma unroll
        for (int i = 0; i < BCH; ++i) {
            int ch = (t + i * 256) & 7;
            bool ok = (s * BK + ch * 8) < K;
            rb[slot][i] = ok ? *reinterpret_cast<const uint4*>(bg[i] + s * BK) : make_uint4(0, 0, 0, 0);
        }
    };
    f32x4 acc[2][2];
#pragma unroll
    for (int i = 0; i < 2; ++i) { acc[i][0] = (f32x4){0, 0, 0, 0}; acc[i][1] = (f32x4){0, 0, 0, 0}; }
#pragma unroll
    for (int d = 0; d < PF; ++d) gload(d, d);
    int fr = lane & 15, fq = lane >> 4;
#pragma unroll
    for (int s = 0; s < NST; ++s) {
        char* buf = ldsbase + (s & 1) * STG;
        const int slot = s % PF;
#pragma unroll
        for (int i = 0; i < ACH; ++i) *reinterpret_cast<uint4*>(buf + aoff[i]) = ra[slot][i];
#pragma unroll
        for (int i = 0; i < BCH; ++i) *reinterpret_cast<uint4*>(buf + boff[i]) = rb[slot][i];
        if (s + PF < NST) gload(slot, s + PF);
        __syncthreads();
#pragma unroll
        for (int kk = 0; kk < 2; ++kk) {
            bf16x8 af[2], bfr[2];
#pragma unroll
            for (int i = 0; i < 2; ++i) {
                int r = wr * 32 + i * 16 + fr;
                af[i] = *reinterpret_cast<const bf16x8*>(buf + r * 128 + (((kk * 4 + fq) ^ (r & 7)) << 4));
            }
#pragma unroll
            for (int j = 0; j < 2; ++j) {
                int r = wc * 32 + j * 16 + fr;
                bfr[j] = *reinterpret_cast<const bf16x8*>(buf + (BM + r) * 128 + (((kk * 4 + fq) ^ (r & 7)) << 4));
            }
#pragma unroll
            for (int i = 0; i < 2; ++i)
#pragma unroll
                for (int j = 0; j < 2; ++j)
                    acc[i][j] = __builtin_amdgcn_mfma_f32_16x16x32_bf16(bfr[j], af[i], acc[i][j], 0, 0, 0);
        }
    }
#pragma unroll
    for (int i = 0; i < 2; ++i)
#pragma unroll
        for (int j = 0; j < 2; ++j) {
            int m = m0 + wr * 32 + i * 16 + fr;
            int n = n0 + wc * 32 + j * 16 + fq * 4;
            if (n < N) {
                u16 o[4];
#pragma unroll
                for (int r = 0; r < 4; ++r) o[r] = f2bf(silu_f(rbf(acc[i][j][r])));
                uint2 pk = make_uint2((uint32_t)o[0] | ((uint32_t)o[1] << 16), (uint32_t)o[2] | ((uint32_t)o[3] << 16));
                *reinterpret_cast<uint2*>(C + (size_t)m * N + n) = pk;
            }
        }
}

template <int K, int N>
__global__ __launch_bounds__(256) void gemm_nt_silu(const u16* __restrict__ A, const u16* __restrict__ Bt,
                                                     u16* __restrict__ C) {
    __shared__ __attribute__((aligned(16))) char lds[2 * 128 * 128];
    int bid = blockIdx.x;
    int per = gridDim.x / 8;
    bid = (bid % 8) * per + bid / 8;
    gemm_tile_silu<K, N>(A, Bt, C, bid, lds);
}

__global__ __launch_bounds__(256) void k3_rowc2r(const float2* __restrict__ XT, const u16* __restrict__ x,
                                                  const u16* __restrict__ g, u16* __restrict__ out) {
    __shared__ float2 smem_tw[512];
    __shared__ float2 smem_buf[4][2][256];
    __shared__ float2 smem_in[256][17];
    int t = threadIdx.x;
    int s0 = blockIdx.x * 16;
    for (int k = t; k < 512; k += 256) {
        float sn, cs;
        sincospif(-(float)k / 256.0f, &sn, &cs);
        smem_tw[k] = make_float2(cs, sn);
    }
    for (int i = t; i < 256 * 16; i += 256) {
        int f = i >> 4, r = i & 15;
        smem_in[f][r] = XT[(size_t)f * S_LEN + s0 + r];
    }
    __syncthreads();
    int wave = t >> 6, lane = t & 63;
    for (int rr = 0; rr < 4; ++rr) {
        int rl = wave * 4 + rr;
        int row = s0 + rl;
        int j = lane;
        float2 v[4];
#pragma unroll
        for (int r = 0; r < 4; ++r) {
            int k = j + r * 64;
            float2 xk, xm;
            if (k == 0) {
                float2 p = smem_in[0][rl];
                xk = make_float2(p.x, 0.f);
                xm = make_float2(p.y, 0.f);
            } else {
                xk = smem_in[k][rl];
                xm = smem_in[256 - k][rl];
            }
            float2 e = make_float2(xk.x + xm.x, xk.y - xm.y);
            float2 o = make_float2(xk.x - xm.x, xk.y + xm.y);
            float2 w = smem_tw[k];
            w.y = -w.y;
            float2 wo = cmul(w, o);
            v[r] = make_float2(e.x - wo.y, e.y + wo.x);
        }
        float2* b0 = smem_buf[wave][0];
        float2* b1 = smem_buf[wave][1];
        fft4<1>(v[0], v[1], v[2], v[3]);
#pragma unroll
        for (int r = 0; r < 4; ++r) b0[j * 4 + r] = v[r];
        __builtin_amdgcn_wave_barrier();
        __builtin_amdgcn_fence(__ATOMIC_SEQ_CST, "wavefront");
#pragma unroll
        for (int r = 0; r < 4; ++r) v[r] = b0[j + r * 64];
#pragma unroll
        for (int r = 1; r < 4; ++r) { float2 w = smem_tw[r * (j & 3) * 32]; w.y = -w.y; v[r] = cmul(v[r], w); }
        fft4<1>(v[0], v[1], v[2], v[3]);
#pragma unroll
        for (int r = 0; r < 4; ++r) b1[(j >> 2) * 16 + (j & 3) + r * 4] = v[r];
        __builtin_amdgcn_wave_barrier();
        __builtin_amdgcn_fence(__ATOMIC_SEQ_CST, "wavefront");
#pragma unroll
        for (int r = 0; r < 4; ++r) v[r] = b1[j + r * 64];
#pragma unroll
        for (int r = 1; r < 4; ++r) { float2 w = smem_tw[r * (j & 15) * 8]; w.y = -w.y; v[r] = cmul(v[r], w); }
        fft4<1>(v[0], v[1], v[2], v[3]);
#pragma unroll
        for (int r = 0; r < 4; ++r) b0[(j >> 4) * 64 + (j & 15) + r * 16] = v[r];
        __builtin_amdgcn_wave_barrier();
        __builtin_amdgcn_fence(__ATOMIC_SEQ_CST, "wavefront");
#pragma unroll
        for (int r = 0; r < 4; ++r) v[r] = b0[j + r * 64];
#pragma unroll
        for (int r = 1; r < 4; ++r) { float2 w = smem_tw[r * j * 2]; w.y = -w.y; v[r] = cmul(v[r], w); }
        fft4<1>(v[0], v[1], v[2], v[3]);
        const float scale = 1.0f / ((float)S_LEN * (float)D_LEN);
        const uint32_t* xr = reinterpret_cast<const uint32_t*>(x + (size_t)row * D_LEN);
        const uint32_t* gr = reinterpret_cast<const uint32_t*>(g + (size_t)row * D_LEN);
        uint32_t* orow = reinterpret_cast<uint32_t*>(out + (size_t)row * D_LEN);
#pragma unroll
        for (int r = 0; r < 4; ++r) {
            int n = j + r * 64;
            uint32_t xp = xr[n], gp = gr[n];
            float s0v = rbf(v[r].x * scale), s1v = rbf(v[r].y * scale);
            float o0 = bf2f((u16)(xp & 0xffff)) + rbf(s0v * bf2f((u16)(gp & 0xffff)));
            float o1 = bf2f((u16)(xp >> 16)) + rbf(s1v * bf2f((u16)(gp >> 16)));
            orow[n] = (uint32_t)f2bf(o0) | ((uint32_t)f2bf(o1) << 16);
        }
        __builtin_amdgcn_wave_barrier();
        __builtin_amdgcn_fence(__ATOMIC_SEQ_CST, "wavefront");
    }
}

extern "C" void kernel_launch(void* const* d_in, const int* in_sizes, int n_in, void* d_out, int out_size, void* d_ws,
                              size_t ws_size, hipStream_t stream) {
    const u16* x = (const u16*)d_in[0];
    const u16* w_r = (const u16*)d_in[1];
    const u16* w_i = (const u16*)d_in[2];
    const u16* w1 = (const u16*)d_in[4];
    const u16* w2 = (const u16*)d_in[5];
    u16* out = (u16*)d_out;
    char* ws = (char*)d_ws;
    u16* xn = (u16*)(ws);
    float2* XT = (float2*)(ws + (2u << 20));
    u16* w1T = (u16*)(ws + (6u << 20));
    u16* w2T = (u16*)(ws + (8u << 20));
    uint32_t* WT = (uint32_t*)(ws + (10u << 20));
    u16* h = (u16*)(ws + (13u << 20));
    u16* g = (u16*)(ws + (19u << 20));

    k1_ln_rowfft<<<K1_ROWBLK + K1_W1BLK + K1_W2BLK + K1_WBLK, 256, 0, stream>>>(x, w_r, w_i, w1, w2, xn, XT, w1T, w2T, WT);
    gemm_nt_silu<512, 1376><<<32 * 22, 256, 0, stream>>>(xn, w1T, h);
    k2_colfft<<<256, 256, 0, stream>>>(XT, WT);
    gemm_nt_silu<1376, 512><<<32 * 8, 256, 0, stream>>>(h, w2T, g);
    k3_rowc2r<<<128, 256, 0, stream>>>(XT, x, g, out);
}
```

```cpp
#include <hip/hip_runtime.h>
#include <hip/hip_bf16.h>
#include <stdint.h>

#define S_LEN 2048
#define D_LEN 512
#define DF 257
#define F_LEN 1376

typedef unsigned short u16;
using bf16x8 = __attribute__((ext_vector_type(8))) short;
using f32x4 = __attribute__((ext_vector_type(4))) float;

__device__ __forceinline__ float bf2f(u16 u) { return __uint_as_float(((unsigned)u) << 16); }
__device__ __forceinline__ u16 f2bf(float f) {
    __hip_bfloat16 b = __float2bfloat16(f);
    return *reinterpret_cast<u16*>(&b);
}
__device__ __forceinline__ float rbf(float f) { return bf2f(f2bf(f)); }
__device__ __forceinline__ float silu_f(float x) { return x / (1.0f + __expf(-x)); }

__device__ __forceinline__ float2 cmul(float2 a, float2 b) {
    return make_float2(a.x * b.x - a.y * b.y, a.x * b.y + a.y * b.x);
}
__device__ __forceinline__ float2 cadd(float2 a, float2 b) { return make_float2(a.x + b.x, a.y + b.y); }
__device__ __forceinline__ float2 csub(float2 a, float2 b) { return make_float2(a.x - b.x, a.y - b.y); }

template <int SGN>
__device__ __forceinline__ void fft4(float2& a, float2& b, float2& c, float2& d) {
    float2 t0 = cadd(a, c), t1 = csub(a, c), t2 = cadd(b, d), t3 = csub(b, d);
    float2 t3r = (SGN < 0) ? make_float2(t3.y, -t3.x) : make_float2(-t3.y, t3.x);
    a = cadd(t0, t2);
    c = csub(t0, t2);
    b = cadd(t1, t3r);
    d = csub(t1, t3r);
}

template <int SGN>
__device__ __forceinline__ void fft8(float2* v) {
    const float r = 0.70710678118654752f;
    float2 a[8];
#pragma unroll
    for (int i = 0; i < 4; ++i) { a[i] = cadd(v[i], v[i + 4]); a[i + 4] = csub(v[i], v[i + 4]); }
    {
        float2 t = a[5];
        a[5] = (SGN < 0) ? make_float2(r * (t.x + t.y), r * (t.y - t.x)) : make_float2(r * (t.x - t.y), r * (t.x + t.y));
        t = a[6];
        a[6] = (SGN < 0) ? make_float2(t.y, -t.x) : make_float2(-t.y, t.x);
        t = a[7];
        a[7] = (SGN < 0) ? make_float2(r * (t.y - t.x), -r * (t.x + t.y)) : make_float2(-r * (t.x + t.y), r * (t.x - t.y));
    }
    fft4<SGN>(a[0], a[1], a[2], a[3]);
    fft4<SGN>(a[4], a[5], a[6], a[7]);
    v[0] = a[0]; v[2] = a[1]; v[4] = a[2]; v[6] = a[3];
    v[1] = a[4]; v[3] = a[5]; v[5] = a[6]; v[7] = a[7];
}

template <int SGN>
__device__ __forceinline__ void fft16(float2* v) {
    const float C1 = 0.92387953251128674f, S1 = 0.38268343236508977f, C2 = 0.70710678118654752f;
#pragma unroll
    for (int i = 0; i < 4; ++i) fft4<SGN>(v[i], v[i + 4], v[i + 8], v[i + 12]);
    const float sg = (SGN < 0) ? -1.0f : 1.0f;
    const float2 w1 = make_float2(C1, sg * S1), w2 = make_float2(C2, sg * C2), w3 = make_float2(S1, sg * C1);
    const float2 w4 = make_float2(0.f, sg), w6 = make_float2(-C2, sg * C2), w9 = make_float2(-C1, -sg * S1);
    v[5] = cmul(v[5], w1);  v[9] = cmul(v[9], w2);   v[13] = cmul(v[13], w3);
    v[6] = cmul(v[6], w2);  v[10] = cmul(v[10], w4); v[14] = cmul(v[14], w6);
    v[7] = cmul(v[7], w3);  v[11] = cmul(v[11], w6); v[15] = cmul(v[15], w9);
#pragma unroll
    for (int k = 0; k < 4; ++k) fft4<SGN>(v[4 * k], v[4 * k + 1], v[4 * k + 2], v[4 * k + 3]);
    float2 o[16];
#pragma unroll
    for (int k = 0; k < 4; ++k)
#pragma unroll
        for (int i = 0; i < 4; ++i) o[k + 4 * i] = v[4 * k + i];
#pragma unroll
    for (int i = 0; i < 16; ++i) v[i] = o[i];
}

#define K1_ROWBLK 128
#define K1_W1BLK (8 * 22)
#define K1_W2BLK (22 * 8)
#define K1_WBLK (32 * 5)

__device__ void transpose_tile16(const u16* __restrict__ in, int R, int C, u16* __restrict__ out, int r0, int c0,
                                 u16 (*tile)[65]) {
    int t = threadIdx.x;
    for (int i = t; i < 64 * 64; i += 256) {
        int r = i >> 6, c = i & 63;
        int gr = r0 + r, gc = c0 + c;
        tile[r][c] = (gr < R && gc < C) ? in[(size_t)gr * C + gc] : (u16)0;
    }
    __syncthreads();
    for (int i = t; i < 64 * 64; i += 256) {
        int c = i >> 6, r = i & 63;
        int gr = r0 + r, gc = c0 + c;
        if (gr < R && gc < C) out[(size_t)gc * R + gr] = tile[r][c];
    }
}

__global__ __launch_bounds__(256) void k1_ln_rowfft(const u16* __restrict__ x, const u16* __restrict__ w_r,
                                                     const u16* __restrict__ w_i, const u16* __restrict__ w1,
                                                     const u16* __restrict__ w2, u16* __restrict__ xn,
                                                     float2* __restrict__ XT, u16* __restrict__ w1T,
                                                     u16* __restrict__ w2T, uint32_t* __restrict__ WT) {
    __shared__ float2 smem_tw[512];
    __shared__ float2 smem_ex[16 * 272];
    __shared__ float2 smem_st[16 * 257 + 8];
    int bid = blockIdx.x;
    int t = threadIdx.x;
    if (bid >= K1_ROWBLK) {
        u16(*tile)[65] = reinterpret_cast<u16(*)[65]>(&smem_st[0]);
        int b = bid - K1_ROWBLK;
        if (b < K1_W1BLK) {
            transpose_tile16(w1, 512, 1376, w1T, (b / 22) * 64, (b % 22) * 64, tile);
            return;
        }
        b -= K1_W1BLK;
        if (b < K1_W2BLK) {
            transpose_tile16(w2, 1376, 512, w2T, (b / 8) * 64, (b % 8) * 64, tile);
            return;
        }
        b -= K1_W2BLK;
        {
            int s0 = (b / 5) * 64, f0 = (b % 5) * 64;
            uint32_t(*tl)[65] = reinterpret_cast<uint32_t(*)[65]>(&smem_st[0]);
            for (int i = t; i < 64 * 64; i += 256) {
                int r = i >> 6, c = i & 63;
                int f = f0 + c;
                uint32_t v = 0;
                if (f < DF) v = (uint32_t)w_r[(size_t)(s0 + r) * DF + f] | ((uint32_t)w_i[(size_t)(s0 + r) * DF + f] << 16);
                tl[r][c] = v;
            }
            __syncthreads();
            for (int i = t; i < 64 * 64; i += 256) {
                int c = i >> 6, r = i & 63;
                int f = f0 + c;
                if (f < DF) WT[(size_t)f * S_LEN + s0 + r] = tl[r][c];
            }
            return;
        }
    }
    const int rl = t >> 4, b = t & 15;
    const int s0 = bid * 16;
    const int row = s0 + rl;
    const uint32_t* xr = reinterpret_cast<const uint32_t*>(x + (size_t)row * D_LEN);
    uint32_t pin[16];
#pragma unroll
    for (int a = 0; a < 16; ++a) pin[a] = xr[16 * a + b];
    for (int k = t; k < 512; k += 256) {
        float sn, cs;
        sincospif(-(float)k / 256.0f, &sn, &cs);
        smem_tw[k] = make_float2(cs, sn);
    }
    float2 v[16];
    float sum = 0.f;
#pragma unroll
    for (int a = 0; a < 16; ++a) {
        v[a] = make_float2(bf2f((u16)(pin[a] & 0xffff)), bf2f((u16)(pin[a] >> 16)));
        sum += v[a].x + v[a].y;
    }
#pragma unroll
    for (int o = 8; o >= 1; o >>= 1) sum += __shfl_xor(sum, o);
    float mean = sum * (1.0f / 512.0f);
    float sq = 0.f;
#pragma unroll
    for (int a = 0; a < 16; ++a) {
        float p = v[a].x - mean, q = v[a].y - mean;
        sq += p * p + q * q;
    }
#pragma unroll
    for (int o = 8; o >= 1; o >>= 1) sq += __shfl_xor(sq, o);
    float var = sq * (1.0f / 512.0f);
    float mean_b = rbf(mean), var_b = rbf(var);
    float den = rbf(sqrtf(rbf(var_b + 1e-5f)));
    uint32_t* xnr = reinterpret_cast<uint32_t*>(xn + (size_t)row * D_LEN);
#pragma unroll
    for (int a = 0; a < 16; ++a) {
        u16 p = f2bf(rbf(v[a].x - mean_b) / den);
        u16 q = f2bf(rbf(v[a].y - mean_b) / den);
        xnr[16 * a + b] = (uint32_t)p | ((uint32_t)q << 16);
        v[a] = make_float2(bf2f(p), bf2f(q));
    }
    __syncthreads();
    fft16<-1>(v);
#pragma unroll
    for (int c = 1; c < 16; ++c) v[c] = cmul(v[c], smem_tw[2 * b * c]);
    float2* ex = &smem_ex[rl * 272];
#pragma unroll
    for (int c = 0; c < 16; ++c) ex[c * 17 + b] = v[c];
    __builtin_amdgcn_wave_barrier();
    __builtin_amdgcn_fence(__ATOMIC_SEQ_CST, "wavefront");
#pragma unroll
    for (int bb = 0; bb < 16; ++bb) v[bb] = ex[b * 17 + bb];
    fft16<-1>(v);
    float2* st = &smem_st[rl * 257];
#pragma unroll
    for (int d = 0; d < 16; ++d) st[b + 16 * d] = v[d];
    __syncthreads();
    for (int i = t; i < 256 * 16; i += 256) {
        int f = i >> 4, r = i & 15;
        const float2* z = &smem_st[r * 257];
        float2 zk = z[f], zm = z[(256 - f) & 255];
        float2 e = make_float2(0.5f * (zk.x + zm.x), 0.5f * (zk.y - zm.y));
        float2 o = make_float2(0.5f * (zk.x - zm.x), 0.5f * (zk.y + zm.y));
        float2 wo = cmul(smem_tw[f], o);
        float2 X = make_float2(e.x + wo.y, e.y - wo.x);
        if (f == 0) X = make_float2(zk.x + zk.y, zk.x - zk.y);
        XT[(size_t)f * S_LEN + s0 + r] = X;
    }
}

#define PADI(i) ((i) + ((i) >> 4))
__device__ __forceinline__ float2 unpack_bf2(uint32_t p) { return make_float2(bf2f((u16)(p & 0xffff)), bf2f((u16)(p >> 16))); }

__device__ __forceinline__ void colfft_body(float2* __restrict__ XT, const uint32_t* __restrict__ WT, int f,
                                            float2* A, float2* B, float2* tw) {
    const int t = threadIdx.x;
    float2* col = XT + (size_t)f * S_LEN;
    float2 u[2][4];
#pragma unroll
    for (int h = 0; h < 2; ++h)
#pragma unroll
        for (int r = 0; r < 4; ++r) u[h][r] = col[t + h * 256 + r * 512];
    uint32_t wp[8], wq[8];
    {
        const uint32_t* wc = WT + (size_t)f * S_LEN;
#pragma unroll
        for (int r = 0; r < 8; ++r) wp[r] = wc[t + r * 256];
        if (f == 0) {
            const uint32_t* wn = WT + (size_t)256 * S_LEN;
#pragma unroll
            for (int r = 0; r < 8; ++r) wq[r] = wn[t + r * 256];
        }
    }
    for (int k = t; k < 2048; k += 256) {
        float sn, cs;
        sincospif(-(float)k / 1024.0f, &sn, &cs);
        tw[k] = make_float2(cs, sn);
    }
#pragma unroll
    for (int h = 0; h < 2; ++h) {
        int jj = t + h * 256;
        fft4<-1>(u[h][0], u[h][1], u[h][2], u[h][3]);
#pragma unroll
        for (int r = 0; r < 4; ++r) A[PADI(jj * 4 + r)] = u[h][r];
    }
    __syncthreads();
    const int j = t;
    float2 v[8];
#pragma unroll
    for (int r = 0; r < 8; ++r) v[r] = A[PADI(j + r * 256)];
#pragma unroll
    for (int r = 1; r < 8; ++r) v[r] = cmul(v[r], tw[r * (j & 3) * 64]);
    fft8<-1>(v);
#pragma unroll
    for (int r = 0; r < 8; ++r) B[PADI((j >> 2) * 32 + (j & 3) + r * 4)] = v[r];
    __syncthreads();
#pragma unroll
    for (int r = 0; r < 8; ++r) v[r] = B[PADI(j + r * 256)];
#pragma unroll
    for (int r = 1; r < 8; ++r) v[r] = cmul(v[r], tw[r * (j & 31) * 8]);
    fft8<-1>(v);
#pragma unroll
    for (int r = 0; r < 8; ++r) A[PADI((j >> 5) * 256 + (j & 31) + r * 32)] = v[r];
    __syncthreads();
#pragma unroll
    for (int r = 0; r < 8; ++r) v[r] = A[PADI(j + r * 256)];
#pragma unroll
    for (int r = 1; r < 8; ++r) v[r] = cmul(v[r], tw[r * j]);
    fft8<-1>(v);
    if (f != 0) {
#pragma unroll
        for (int r = 0; r < 8; ++r) v[r] = cmul(v[r], unpack_bf2(wp[r]));
    } else {
#pragma unroll
        for (int r = 0; r < 8; ++r) B[PADI(j + r * 256)] = v[r];
        uint32_t* w0s = reinterpret_cast<uint32_t*>(A);
        uint32_t* wns = reinterpret_cast<uint32_t*>(A) + 2048;
        __syncthreads();
#pragma unroll
        for (int r = 0; r < 8; ++r) { w0s[j + r * 256] = wp[r]; wns[j + r * 256] = wq[r]; }
        __syncthreads();
#pragma unroll
        for (int r = 0; r < 8; ++r) {
            int k = j + r * 256, km = (2048 - k) & 2047;
            float2 zk = v[r], zm = B[PADI(km)];
            float2 f0k = make_float2(0.5f * (zk.x + zm.x), 0.5f * (zk.y - zm.y));
            float2 fnk = make_float2(0.5f * (zk.y + zm.y), -0.5f * (zk.x - zm.x));
            float2 f0m = make_float2(f0k.x, -f0k.y);
            float2 fnm = make_float2(fnk.x, -fnk.y);
            float2 y0k = cmul(f0k, unpack_bf2(wp[r])), y0m = cmul(f0m, unpack_bf2(w0s[km]));
            float2 ynk = cmul(fnk, unpack_bf2(wq[r])), ynm = cmul(fnm, unpack_bf2(wns[km]));
            float2 h0 = make_float2(0.5f * (y0k.x + y0m.x), 0.5f * (y0k.y - y0m.y));
            float2 hn = make_float2(0.5f * (ynk.x + ynm.x), 0.5f * (ynk.y - ynm.y));
            v[r] = make_float2(h0.x - hn.y, h0.y + hn.x);
        }
        __syncthreads();
    }
    fft8<1>(v);
#pragma unroll
    for (int r = 0; r < 8; ++r) B[PADI(j * 8 + r)] = v[r];
    __syncthreads();
#pragma unroll
    for (int r = 0; r < 8; ++r) v[r] = B[PADI(j + r * 256)];
#pragma unroll
    for (int r = 1; r < 8; ++r) { float2 w = tw[r * (j & 7) * 32]; w.y = -w.y; v[r] = cmul(v[r], w); }
    fft8<1>(v);
#pragma unroll
    for (int r = 0; r < 8; ++r) A[PADI((j >> 3) * 64 + (j & 7) + r * 8)] = v[r];
    __syncthreads();
#pragma unroll
    for (int r = 0; r < 8; ++r) v[r] = A[PADI(j + r * 256)];
#pragma unroll
    for (int r = 1; r < 8; ++r) { float2 w = tw[r * (j & 63) * 4]; w.y = -w.y; v[r] = cmul(v[r], w); }
    fft8<1>(v);
#pragma unroll
    for (int r = 0; r < 8; ++r) B[PADI((j >> 6) * 512 + (j & 63) + r * 64)] = v[r];
    __syncthreads();
#pragma unroll
    for (int h = 0; h < 2; ++h) {
        int jj = j + h * 256;
        float2 q[4];
#pragma unroll
        for (int r = 0; r < 4; ++r) q[r] = B[PADI(jj + r * 512)];
#pragma unroll
        for (int r = 1; r < 4; ++r) { float2 w = tw[r * jj]; w.y = -w.y; q[r] = cmul(q[r], w); }
        fft4<1>(q[0], q[1], q[2], q[3]);
#pragma unroll
        for (int r = 0; r < 4; ++r) col[jj + r * 512] = q[r];
    }
}

__global__ __launch_bounds__(256) void k2_colfft(float2* __restrict__ XT, const uint32_t* __restrict__ WT) {
    __shared__ float2 A[2048 + 128];
    __shared__ float2 B[2048 + 128];
    __shared__ float2 tw[2048];
    colfft_body(XT, WT, blockIdx.x, A, B, tw);
}

template <int K, int N>
__device__ __forceinline__ void gemm_tile_silu(const u16* __restrict__ A, const u16* __restrict__ Bt,
                                               u16* __restrict__ C, int bid, char* ldsbase) {
    constexpr int BM = 64, BN = 64, BK = 64, PF = 3;
    constexpr int NST = (K + BK - 1) / BK;
    constexpr int ACH = 2, BCH = 2;
    constexpr int STG = (BM + BN) * 128;
    int t = threadIdx.x, wave = t >> 6, lane = t & 63;
    constexpr int nb = (N + BN - 1) / BN;
    int bm = bid / nb, bn = bid % nb;
    int m0 = bm * BM, n0 = bn * BN;
    int wr = wave >> 1, wc = wave & 1;
    const u16* ag[ACH];
    const u16* bg[BCH];
    int aoff[ACH], boff[BCH];
#pragma unroll
    for (int i = 0; i < ACH; ++i) {
        int c = t + i * 256, r = c >> 3, ch = c & 7;
        ag[i] = A + (size_t)(m0 + r) * K + ch * 8;
        aoff[i] = r * 128 + ((ch ^ (r & 7)) << 4);
    }
#pragma unroll
    for (int i = 0; i < BCH; ++i) {
        int c = t + i * 256, r = c >> 3, ch = c & 7;
        int gr = n0 + r;
        gr = gr < N ? gr : N - 1;
        bg[i] = Bt + (size_t)gr * K + ch * 8;
        boff[i] = (BM + r) * 128 + ((ch ^ (r & 7)) << 4);
    }
    uint4 ra[PF][ACH], rb[PF][BCH];
    auto gload = [&](int slot, int s) {
#pragma unroll
        for (int i = 0; i < ACH; ++i) {
            int ch = (t + i * 256) & 7;
            bool ok = (s * BK + ch * 8) < K;
            ra[slot][i] = ok ? *reinterpret_cast<const uint4*>(ag[i] + s * BK) : make_uint4(0, 0, 0, 0);
        }
#pragma unroll
        for (int i = 0; i < BCH; ++i) {
            int ch = (t + i * 256) & 7;
            bool ok = (s * BK + ch * 8) < K;
            rb[slot][i] = ok ? *reinterpret_cast<const uint4*>(bg[i] + s * BK) : make_uint4(0, 0, 0, 0);
        }
    };
    f32x4 acc[2][2];
#pragma unroll
    for (int i = 0; i < 2; ++i) { acc[i][0] = (f32x4){0, 0, 0, 0}; acc[i][1] = (f32x4){0, 0, 0, 0}; }
#pragma unroll
    for (int d = 0; d < PF; ++d) gload(d, d);
    int fr = lane & 15, fq = lane >> 4;
#pragma unroll
    for (int s = 0; s < NST; ++s) {
        char* buf = ldsbase + (s & 1) * STG;
        const int slot = s % PF;
#pragma unroll
        for (int i = 0; i < ACH; ++i) *reinterpret_cast<uint4*>(buf + aoff[i]) = ra[slot][i];
#pragma unroll
        for (int i = 0; i < BCH; ++i) *reinterpret_cast<uint4*>(buf + boff[i]) = rb[slot][i];
        if (s + PF < NST) gload(slot, s + PF);
        __syncthreads();
#pragma unroll
        for (int kk = 0; kk < 2; ++kk) {
            bf16x8 af[2], bfr[2];
#pragma unroll
            for (int i = 0; i < 2; ++i) {
                int r = wr * 32 + i * 16 + fr;
                af[i] = *reinterpret_cast<const bf16x8*>(buf + r * 128 + (((kk * 4 + fq) ^ (r & 7)) << 4));
            }
#pragma unroll
            for (int j = 0; j < 2; ++j) {
                int r = wc * 32 + j * 16 + fr;
                bfr[j] = *reinterpret_cast<const bf16x8*>(buf + (BM + r) * 128 + (((kk * 4 + fq) ^ (r & 7)) << 4));
            }
#pragma unroll
            for (int i = 0; i < 2; ++i)
#pragma unroll
                for (int j = 0; j < 2; ++j)
                    acc[i][j] = __builtin_amdgcn_mfma_f32_16x16x32_bf16(bfr[j], af[i], acc[i][j], 0, 0, 0);
        }
    }
#pragma unroll
    for (int i = 0; i < 2; ++i)
#pragma unroll
        for (int j = 0; j < 2; ++j) {
            int m = m0 + wr * 32 + i * 16 + fr;
            int n = n0 + wc * 32 + j * 16 + fq * 4;
            if (n < N) {
                u16 o[4];
#pragma unroll
                for (int r = 0; r < 4; ++r) o[r] = f2bf(silu_f(rbf(acc[i][j][r])));
                uint2 pk = make_uint2((uint32_t)o[0] | ((uint32_t)o[1] << 16), (uint32_t)o[2] | ((uint32_t)o[3] << 16));
                *reinterpret_cast<uint2*>(C + (size_t)m * N + n) = pk;
            }
        }
}

template <int K, int N>
__global__ __launch_bounds__(256) void gemm_nt_silu(const u16* __restrict__ A, const u16* __restrict__ Bt,
                                                     u16* __restrict__ C) {
    __shared__ __attribute__((aligned(16))) char lds[2 * 128 * 128];
    int bid = blockIdx.x;
    int per = gridDim.x / 8;
    bid = (bid % 8) * per + bid / 8;
    gemm_tile_silu<K, N>(A, Bt, C, bid, lds);
}

__global__ __launch_bounds__(256) void k3_rowc2r(const float2* __restrict__ XT, const u16* __restrict__ x,
                                                  const u16* __restrict__ g, u16* __restrict__ out) {
    __shared__ float2 smem_tw[512];
    __shared__ float2 smem_ex[16 * 272];
    __shared__ float2 smem_st[16 * 257 + 8];
    const int t = threadIdx.x;
    const int s0 = blockIdx.x * 16;
    {
        float2 tmp[16];
#pragma unroll
        for (int it = 0; it < 16; ++it) {
            int i = t + it * 256;
            tmp[it] = XT[(size_t)(i >> 4) * S_LEN + s0 + (i & 15)];
        }
        for (int k = t; k < 512; k += 256) {
            float sn, cs;
            sincospif(-(float)k / 256.0f, &sn, &cs);
            smem_tw[k] = make_float2(cs, sn);
        }
#pragma unroll
        for (int it = 0; it < 16; ++it) {
            int i = t + it * 256;
            smem_st[(i & 15) * 257 + (i >> 4)] = tmp[it];
        }
    }
    const int rl = t >> 4, b = t & 15;
    const int row = s0 + rl;
    const uint32_t* xr = reinterpret_cast<const uint32_t*>(x + (size_t)row * D_LEN);
    const uint32_t* gr = reinterpret_cast<const uint32_t*>(g + (size_t)row * D_LEN);
    uint32_t xp[16], gp[16];
#pragma unroll
    for (int d = 0; d < 16; ++d) { xp[d] = xr[b + 16 * d]; gp[d] = gr[b + 16 * d]; }
    __syncthreads();
    float2 v[16];
    const float2* z = &smem_st[rl * 257];
#pragma unroll
    for (int a = 0; a < 16; ++a) {
        int k = 16 * a + b;
        float2 xk = z[k], xm = z[(256 - k) & 255];
        if (k == 0) { xm = make_float2(xk.y, 0.f); xk = make_float2(xk.x, 0.f); }
        float2 e = make_float2(xk.x + xm.x, xk.y - xm.y);
        float2 o = make_float2(xk.x - xm.x, xk.y + xm.y);
        float2 w = smem_tw[k];
        w.y = -w.y;
        float2 wo = cmul(w, o);
        v[a] = make_float2(e.x - wo.y, e.y + wo.x);
    }
    fft16<1>(v);
#pragma unroll
    for (int c = 1; c < 16; ++c) { float2 w = smem_tw[2 * b * c]; w.y = -w.y; v[c] = cmul(v[c], w); }
    float2* ex = &smem_ex[rl * 272];
#pragma unroll
    for (int c = 0; c < 16; ++c) ex[c * 17 + b] = v[c];
    __builtin_amdgcn_wave_barrier();
    __builtin_amdgcn_fence(__ATOMIC_SEQ_CST, "wavefront");
#pragma unroll
    for (int bb = 0; bb < 16; ++bb) v[bb] = ex[b * 17 + bb];
    fft16<1>(v);
    const float scale = 1.0f / ((float)S_LEN * (float)D_LEN);
    uint32_t* orow = reinterpret_cast<uint32_t*>(out + (size_t)row * D_LEN);
#pragma unroll
    for (int d = 0; d < 16; ++d) {
        float s0v = rbf(v[d].x * scale), s1v = rbf(v[d].y * scale);
        float o0 = bf2f((u16)(xp[d] & 0xffff)) + rbf(s0v * bf2f((u16)(gp[d] & 0xffff)));
        float o1 = bf2f((u16)(xp[d] >> 16)) + rbf(s1v * bf2f((u16)(gp[d] >> 16)));
        orow[b + 16 * d] = (uint32_t)f2bf(o0) | ((uint32_t)f2bf(o1) << 16);
    }
}

extern "C" void kernel_launch(void* const* d_in, const int* in_sizes, int n_in, void* d_out, int out_size, void* d_ws,
                              size_t ws_size, hipStream_t stream) {
    const u16* x = (const u16*)d_in[0];
    const u16* w_r = (const u16*)d_in[1];
    const u16* w_i = (const u16*)d_in[2];
    const u16* w1 = (const u16*)d_in[4];
    const u16* w2 = (const u16*)d_in[5];
    u16* out = (u16*)d_out;
    char* ws = (char*)d_ws;
    u16* xn = (u16*)(ws);
    float2* XT = (float2*)(ws + (2u << 20));
    u16* w1T = (u16*)(ws + (6u << 20));
    u16* w2T = (u16*)(ws + (8u << 20));
    uint32_t* WT = (uint32_t*)(ws + (10u << 20));
    u16* h = (u16*)(ws + (13u << 20));
    u16* g = (u16*)(ws + (19u << 20));

    k1_ln_rowfft<<<K1_ROWBLK + K1_W1BLK + K1_W2BLK + K1_WBLK, 256, 0, stream>>>(x, w_r, w_i, w1, w2, xn, XT, w1T, w2T, WT);
    gemm_nt_silu<512, 1376><<<32 * 22, 256, 0, stream>>>(xn, w1T, h);
    k2_colfft<<<256, 256, 0, stream>>>(XT, WT);
    gemm_nt_silu<1376, 512><<<32 * 8, 256, 0, stream>>>(h, w2T, g);
    k3_rowc2r<<<128, 256, 0, stream>>>(XT, x, g, out);
}
```

```cpp
#include <hip/hip_runtime.h>
#include <hip/hip_bf16.h>
#include <stdint.h>

#define S_LEN 2048
#define D_LEN 512
#define DF 257
#define F_LEN 1376
#define XP 2080

typedef unsigned short u16;
using bf16x8 = __attribute__((ext_vector_type(8))) short;
using f32x4 = __attribute__((ext_vector_type(4))) float;

__device__ __forceinline__ float bf2f(u16 u) { return __uint_as_float(((unsigned)u) << 16); }
__device__ __forceinline__ u16 f2bf(float f) {
    __hip_bfloat16 b = __float2bfloat16(f);
    return *reinterpret_cast<u16*>(&b);
}
__device__ __forceinline__ float rbf(float f) { return bf2f(f2bf(f)); }
__device__ __forceinline__ float silu_f(float x) { return x / (1.0f + __expf(-x)); }

__device__ __forceinline__ float2 cmul(float2 a, float2 b) {
    return make_float2(a.x * b.x - a.y * b.y, a.x * b.y + a.y * b.x);
}
__device__ __forceinline__ float2 cadd(float2 a, float2 b) { return make_float2(a.x + b.x, a.y + b.y); }
__device__ __forceinline__ float2 csub(float2 a, float2 b) { return make_float2(a.x - b.x, a.y - b.y); }

template <int SGN>
__device__ __forceinline__ void fft4(float2& a, float2& b, float2& c, float2& d) {
    float2 t0 = cadd(a, c), t1 = csub(a, c), t2 = cadd(b, d), t3 = csub(b, d);
    float2 t3r = (SGN < 0) ? make_float2(t3.y, -t3.x) : make_float2(-t3.y, t3.x);
    a = cadd(t0, t2);
    c = csub(t0, t2);
    b = cadd(t1, t3r);
    d = csub(t1, t3r);
}

template <int SGN>
__device__ __forceinline__ void fft8(float2* v) {
    const float r = 0.70710678118654752f;
    float2 a[8];
#pragma unroll
    for (int i = 0; i < 4; ++i) { a[i] = cadd(v[i], v[i + 4]); a[i + 4] = csub(v[i], v[i + 4]); }
    {
        float2 t = a[5];
        a[5] = (SGN < 0) ? make_float2(r * (t.x + t.y), r * (t.y - t.x)) : make_float2(r * (t.x - t.y), r * (t.x + t.y));
        t = a[6];
        a[6] = (SGN < 0) ? make_float2(t.y, -t.x) : make_float2(-t.y, t.x);
        t = a[7];
        a[7] = (SGN < 0) ? make_float2(r * (t.y - t.x), -r * (t.x + t.y)) : make_float2(-r * (t.x + t.y), r * (t.x - t.y));
    }
    fft4<SGN>(a[0], a[1], a[2], a[3]);
    fft4<SGN>(a[4], a[5], a[6], a[7]);
    v[0] = a[0]; v[2] = a[1]; v[4] = a[2]; v[6] = a[3];
    v[1] = a[4]; v[3] = a[5]; v[5] = a[6]; v[7] = a[7];
}

template <int SGN>
__device__ __forceinline__ void fft16(float2* v) {
    const float C1 = 0.92387953251128674f, S1 = 0.38268343236508977f, C2 = 0.70710678118654752f;
#pragma unroll
    for (int i = 0; i < 4; ++i) fft4<SGN>(v[i], v[i + 4], v[i + 8], v[i + 12]);
    const float sg = (SGN < 0) ? -1.0f : 1.0f;
    const float2 w1 = make_float2(C1, sg * S1), w2 = make_float2(C2, sg * C2), w3 = make_float2(S1, sg * C1);
    const float2 w4 = make_float2(0.f, sg), w6 = make_float2(-C2, sg * C2), w9 = make_float2(-C1, -sg * S1);
    v[5] = cmul(v[5], w1);  v[9] = cmul(v[9], w2);   v[13] = cmul(v[13], w3);
    v[6] = cmul(v[6], w2);  v[10] = cmul(v[10], w4); v[14] = cmul(v[14], w6);
    v[7] = cmul(v[7], w3);  v[11] = cmul(v[11], w6); v[15] = cmul(v[15], w9);
#pragma unroll
    for (int k = 0; k < 4; ++k) fft4<SGN>(v[4 * k], v[4 * k + 1], v[4 * k + 2], v[4 * k + 3]);
    float2 o[16];
#pragma unroll
    for (int k = 0; k < 4; ++k)
#pragma unroll
        for (int i = 0; i < 4; ++i) o[k + 4 * i] = v[4 * k + i];
#pragma unroll
    for (int i = 0; i < 16; ++i) v[i] = o[i];
}

#define K1_ROWBLK 128
#define K1_W1BLK (8 * 22)
#define K1_W2BLK (22 * 8)
#define K1_WBLK (32 * 5)

__device__ __forceinline__ void transpose_tile16(const u16* __restrict__ in, int R, int C, u16* __restrict__ out,
                                                 int r0, int c0, u16* tile  ) {
    const int t = threadIdx.x;
    uint4 ld[2];
#pragma unroll
    for (int i = 0; i < 2; ++i) {
        int r = (t >> 3) + 32 * i, c8 = (t & 7) * 8;
        bool ok = (r0 + r < R) && (c0 + c8 < C);
        ld[i] = ok ? *reinterpret_cast<const uint4*>(in + (size_t)(r0 + r) * C + c0 + c8) : make_uint4(0, 0, 0, 0);
    }
#pragma unroll
    for (int i = 0; i < 2; ++i) {
        int r = (t >> 3) + 32 * i, c8 = (t & 7) * 8;
        uint32_t* q = reinterpret_cast<uint32_t*>(tile + r * 66 + c8);
        q[0] = ld[i].x; q[1] = ld[i].y; q[2] = ld[i].z; q[3] = ld[i].w;
    }
    __syncthreads();
#pragma unroll
    for (int i = 0; i < 2; ++i) {
        int c = (t >> 3) + 32 * i, r8 = (t & 7) * 8;
        u16 e[8];
#pragma unroll
        for (int q = 0; q < 8; ++q) e[q] = tile[(r8 + q) * 66 + c];
        uint4 o = make_uint4((uint32_t)e[0] | ((uint32_t)e[1] << 16), (uint32_t)e[2] | ((uint32_t)e[3] << 16),
                             (uint32_t)e[4] | ((uint32_t)e[5] << 16), (uint32_t)e[6] | ((uint32_t)e[7] << 16));
        if ((c0 + c < C) && (r0 + r8 < R)) *reinterpret_cast<uint4*>(out + (size_t)(c0 + c) * R + r0 + r8) = o;
    }
}

__global__ __launch_bounds__(256) void k1_ln_rowfft(const u16* __restrict__ x, const u16* __restrict__ w_r,
                                                     const u16* __restrict__ w_i, const u16* __restrict__ w1,
                                                     const u16* __restrict__ w2, u16* __restrict__ xn,
                                                     float2* __restrict__ XT, u16* __restrict__ w1T,
                                                     u16* __restrict__ w2T, uint32_t* __restrict__ WT) {
    __shared__ float2 smem_tw[512];
    __shared__ float2 smem_ex[16 * 272];
    __shared__ float2 smem_st[16 * 257 + 8];
    int bid = blockIdx.x;
    int t = threadIdx.x;
    if (bid >= K1_ROWBLK) {
        u16* tile = reinterpret_cast<u16*>(&smem_st[0]);
        int b = bid - K1_ROWBLK;
        if (b < K1_W1BLK) {
            transpose_tile16(w1, 512, 1376, w1T, (b / 22) * 64, (b % 22) * 64, tile);
            return;
        }
        b -= K1_W1BLK;
        if (b < K1_W2BLK) {
            transpose_tile16(w2, 1376, 512, w2T, (b / 8) * 64, (b % 8) * 64, tile);
            return;
        }
        b -= K1_W2BLK;
        {
            int s0 = (b / 5) * 64, f0 = (b % 5) * 64;
            uint32_t* tl = reinterpret_cast<uint32_t*>(&smem_st[0]);
            u16 lr[16], li[16];
            const int c = t & 63, rb = t >> 6;
            const int f = f0 + c;
            const int fc = f < DF ? f : DF - 1;
#pragma unroll
            for (int i = 0; i < 16; ++i) {
                int r = rb + 4 * i;
                lr[i] = w_r[(size_t)(s0 + r) * DF + fc];
                li[i] = w_i[(size_t)(s0 + r) * DF + fc];
            }
#pragma unroll
            for (int i = 0; i < 16; ++i) tl[(rb + 4 * i) * 65 + c] = (uint32_t)lr[i] | ((uint32_t)li[i] << 16);
            __syncthreads();
#pragma unroll
            for (int i = 0; i < 4; ++i) {
                int cc = (t >> 4) + 16 * i, r4 = (t & 15) * 4;
                uint4 o = make_uint4(tl[r4 * 65 + cc], tl[(r4 + 1) * 65 + cc], tl[(r4 + 2) * 65 + cc], tl[(r4 + 3) * 65 + cc]);
                if (f0 + cc < DF) *reinterpret_cast<uint4*>(WT + (size_t)(f0 + cc) * S_LEN + s0 + r4) = o;
            }
            return;
        }
    }
    const int rl = t >> 4, b = t & 15;
    const int s0 = bid * 16;
    const int row = s0 + rl;
    const uint32_t* xr = reinterpret_cast<const uint32_t*>(x + (size_t)row * D_LEN);
    uint32_t pin[16];
#pragma unroll
    for (int a = 0; a < 16; ++a) pin[a] = xr[16 * a + b];
    for (int k = t; k < 512; k += 256) {
        float sn, cs;
        sincospif(-(float)k / 256.0f, &sn, &cs);
        smem_tw[k] = make_float2(cs, sn);
    }
    float2 v[16];
    float sum = 0.f;
#pragma unroll
    for (int a = 0; a < 16; ++a) {
        v[a] = make_float2(bf2f((u16)(pin[a] & 0xffff)), bf2f((u16)(pin[a] >> 16)));
        sum += v[a].x + v[a].y;
    }
#pragma unroll
    for (int o = 8; o >= 1; o >>= 1) sum += __shfl_xor(sum, o);
    float mean = sum * (1.0f / 512.0f);
    float sq = 0.f;
#pragma unroll
    for (int a = 0; a < 16; ++a) {
        float p = v[a].x - mean, q = v[a].y - mean;
        sq += p * p + q * q;
    }
#pragma unroll
    for (int o = 8; o >= 1; o >>= 1) sq += __shfl_xor(sq, o);
    float var = sq * (1.0f / 512.0f);
    float mean_b = rbf(mean), var_b = rbf(var);
    float den = rbf(sqrtf(rbf(var_b + 1e-5f)));
    uint32_t* xnr = reinterpret_cast<uint32_t*>(xn + (size_t)row * D_LEN);
#pragma unroll
    for (int a = 0; a < 16; ++a) {
        u16 p = f2bf(rbf(v[a].x - mean_b) / den);
        u16 q = f2bf(rbf(v[a].y - mean_b) / den);
        xnr[16 * a + b] = (uint32_t)p | ((uint32_t)q << 16);
        v[a] = make_float2(bf2f(p), bf2f(q));
    }
    __syncthreads();
    fft16<-1>(v);
#pragma unroll
    for (int c = 1; c < 16; ++c) v[c] = cmul(v[c], smem_tw[2 * b * c]);
    float2* ex = &smem_ex[rl * 272];
#pragma unroll
    for (int c = 0; c < 16; ++c) ex[c * 17 + b] = v[c];
    __builtin_amdgcn_wave_barrier();
    __builtin_amdgcn_fence(__ATOMIC_SEQ_CST, "wavefront");
#pragma unroll
    for (int bb = 0; bb < 16; ++bb) v[bb] = ex[b * 17 + bb];
    fft16<-1>(v);
    float2* st = &smem_st[rl * 257];
#pragma unroll
    for (int d = 0; d < 16; ++d) st[b + 16 * d] = v[d];
    __syncthreads();
    for (int i = t; i < 256 * 16; i += 256) {
        int f = i >> 4, r = i & 15;
        const float2* z = &smem_st[r * 257];
        float2 zk = z[f], zm = z[(256 - f) & 255];
        float2 e = make_float2(0.5f * (zk.x + zm.x), 0.5f * (zk.y - zm.y));
        float2 o = make_float2(0.5f * (zk.x - zm.x), 0.5f * (zk.y + zm.y));
        float2 wo = cmul(smem_tw[f], o);
        float2 X = make_float2(e.x + wo.y, e.y - wo.x);
        if (f == 0) X = make_float2(zk.x + zk.y, zk.x - zk.y);
        XT[(size_t)f * XP + s0 + r] = X;
    }
}

#define PADI(i) ((i) + ((i) >> 4))
__device__ __forceinline__ float2 unpack_bf2(uint32_t p) { return make_float2(bf2f((u16)(p & 0xffff)), bf2f((u16)(p >> 16))); }

__device__ __forceinline__ void colfft_body(float2* __restrict__ XT, const uint32_t* __restrict__ WT, int f,
                                            float2* A, float2* B, float2* tw) {
    const int t = threadIdx.x;
    float2* col = XT + (size_t)f * XP;
    float2 u[2][4];
#pragma unroll
    for (int h = 0; h < 2; ++h)
#pragma unroll
        for (int r = 0; r < 4; ++r) u[h][r] = col[t + h * 256 + r * 512];
    uint32_t wp[8], wq[8];
    {
        const uint32_t* wc = WT + (size_t)f * S_LEN;
#pragma unroll
        for (int r = 0; r < 8; ++r) wp[r] = wc[t + r * 256];
        if (f == 0) {
            const uint32_t* wn = WT + (size_t)256 * S_LEN;
#pragma unroll
            for (int r = 0; r < 8; ++r) wq[r] = wn[t + r * 256];
        }
    }
    for (int k = t; k < 2048; k += 256) {
        float sn, cs;
        sincospif(-(float)k / 1024.0f, &sn, &cs);
        tw[k] = make_float2(cs, sn);
    }
#pragma unroll
    for (int h = 0; h < 2; ++h) {
        int jj = t + h * 256;
        fft4<-1>(u[h][0], u[h][1], u[h][2], u[h][3]);
#pragma unroll
        for (int r = 0; r < 4; ++r) A[PADI(jj * 4 + r)] = u[h][r];
    }
    __syncthreads();
    const int j = t;
    float2 v[8];
#pragma unroll
    for (int r = 0; r < 8; ++r) v[r] = A[PADI(j + r * 256)];
#pragma unroll
    for (int r = 1; r < 8; ++r) v[r] = cmul(v[r], tw[r * (j & 3) * 64]);
    fft8<-1>(v);
#pragma unroll
    for (int r = 0; r < 8; ++r) B[PADI((j >> 2) * 32 + (j & 3) + r * 4)] = v[r];
    __syncthreads();
#pragma unroll
    for (int r = 0; r < 8; ++r) v[r] = B[PADI(j + r * 256)];
#pragma unroll
    for (int r = 1; r < 8; ++r) v[r] = cmul(v[r], tw[r * (j & 31) * 8]);
    fft8<-1>(v);
#pragma unroll
    for (int r = 0; r < 8; ++r) A[PADI((j >> 5) * 256 + (j & 31) + r * 32)] = v[r];
    __syncthreads();
#pragma unroll
    for (int r = 0; r < 8; ++r) v[r] = A[PADI(j + r * 256)];
#pragma unroll
    for (int r = 1; r < 8; ++r) v[r] = cmul(v[r], tw[r * j]);
    fft8<-1>(v);
    if (f != 0) {
#pragma unroll
        for (int r = 0; r < 8; ++r) v[r] = cmul(v[r], unpack_bf2(wp[r]));
    } else {
#pragma unroll
        for (int r = 0; r < 8; ++r) B[PADI(j + r * 256)] = v[r];
        uint32_t* w0s = reinterpret_cast<uint32_t*>(A);
        uint32_t* wns = reinterpret_cast<uint32_t*>(A) + 2048;
        __syncthreads();
#pragma unroll
        for (int r = 0; r < 8; ++r) { w0s[j + r * 256] = wp[r]; wns[j + r * 256] = wq[r]; }
        __syncthreads();
#pragma unroll
        for (int r = 0; r < 8; ++r) {
            int k = j + r * 256, km = (2048 - k) & 2047;
            float2 zk = v[r], zm = B[PADI(km)];
            float2 f0k = make_float2(0.5f * (zk.x + zm.x), 0.5f * (zk.y - zm.y));
            float2 fnk = make_float2(0.5f * (zk.y + zm.y), -0.5f * (zk.x - zm.x));
            float2 f0m = make_float2(f0k.x, -f0k.y);
            float2 fnm = make_float2(fnk.x, -fnk.y);
            float2 y0k = cmul(f0k, unpack_bf2(wp[r])), y0m = cmul(f0m, unpack_bf2(w0s[km]));
            float2 ynk = cmul(fnk, unpack_bf2(wq[r])), ynm = cmul(fnm, unpack_bf2(wns[km]));
            float2 h0 = make_float2(0.5f * (y0k.x + y0m.x), 0.5f * (y0k.y - y0m.y));
            float2 hn = make_float2(0.5f * (ynk.x + ynm.x), 0.5f * (ynk.y - ynm.y));
            v[r] = make_float2(h0.x - hn.y, h0.y + hn.x);
        }
        __syncthreads();
    }
    fft8<1>(v);
#pragma unroll
    for (int r = 0; r < 8; ++r) B[PADI(j * 8 + r)] = v[r];
    __syncthreads();
#pragma unroll
    for (int r = 0; r < 8; ++r) v[r] = B[PADI(j + r * 256)];
#pragma unroll
    for (int r = 1; r < 8; ++r) { float2 w = tw[r * (j & 7) * 32]; w.y = -w.y; v[r] = cmul(v[r], w); }
    fft8<1>(v);
#pragma unroll
    for (int r = 0; r < 8; ++r) A[PADI((j >> 3) * 64 + (j & 7) + r * 8)] = v[r];
    __syncthreads();
#pragma unroll
    for (int r = 0; r < 8; ++r) v[r] = A[PADI(j + r * 256)];
#pragma unroll
    for (int r = 1; r < 8; ++r) { float2 w = tw[r * (j & 63) * 4]; w.y = -w.y; v[r] = cmul(v[r], w); }
    fft8<1>(v);
#pragma unroll
    for (int r = 0; r < 8; ++r) B[PADI((j >> 6) * 512 + (j & 63) + r * 64)] = v[r];
    __syncthreads();
#pragma unroll
    for (int h = 0; h < 2; ++h) {
        int jj = j + h * 256;
        float2 q[4];
#pragma unroll
        for (int r = 0; r < 4; ++r) q[r] = B[PADI(jj + r * 512)];
#pragma unroll
        for (int r = 1; r < 4; ++r) { float2 w = tw[r * jj]; w.y = -w.y; q[r] = cmul(q[r], w); }
        fft4<1>(q[0], q[1], q[2], q[3]);
#pragma unroll
        for (int r = 0; r < 4; ++r) col[jj + r * 512] = q[r];
    }
}

__global__ __launch_bounds__(256) void k2_colfft(float2* __restrict__ XT, const uint32_t* __restrict__ WT) {
    __shared__ float2 A[2048 + 128];
    __shared__ float2 B[2048 + 128];
    __shared__ float2 tw[2048];
    colfft_body(XT, WT, blockIdx.x, A, B, tw);
}

template <int K, int N>
__device__ __forceinline__ void gemm_tile_silu(const u16* __restrict__ A, const u16* __restrict__ Bt,
                                               u16* __restrict__ C, int bid, char* ldsbase) {
    constexpr int BM = 64, BN = 64, BK = 64, PF = 3;
    constexpr int NST = (K + BK - 1) / BK;
    constexpr int ACH = 2, BCH = 2;
    constexpr int STG = (BM + BN) * 128;
    int t = threadIdx.x, wave = t >> 6, lane = t & 63;
    constexpr int nb = (N + BN - 1) / BN;
    int bm = bid / nb, bn = bid % nb;
    int m0 = bm * BM, n0 = bn * BN;
    int wr = wave >> 1, wc = wave & 1;
    const u16* ag[ACH];
    const u16* bg[BCH];
    int aoff[ACH], boff[BCH];
#pragma unroll
    for (int i = 0; i < ACH; ++i) {
        int c = t + i * 256, r = c >> 3, ch = c & 7;
        ag[i] = A + (size_t)(m0 + r) * K + ch * 8;
        aoff[i] = r * 128 + ((ch ^ (r & 7)) << 4);
    }
#pragma unroll
    for (int i = 0; i < BCH; ++i) {
        int c = t + i * 256, r = c >> 3, ch = c & 7;
        int gr = n0 + r;
        gr = gr < N ? gr : N - 1;
        bg[i] = Bt + (size_t)gr * K + ch * 8;
        boff[i] = (BM + r) * 128 + ((ch ^ (r & 7)) << 4);
    }
    uint4 ra[PF][ACH], rb[PF][BCH];
    auto gload = [&](int slot, int s) {
#pragma unroll
        for (int i = 0; i < ACH; ++i) {
            int ch = (t + i * 256) & 7;
            bool ok = (s * BK + ch * 8) < K;
            ra[slot][i] = ok ? *reinterpret_cast<const uint4*>(ag[i] + s * BK) : make_uint4(0, 0, 0, 0);
        }
#pragma unroll
        for (int i = 0; i < BCH; ++i) {
            int ch = (t + i * 256) & 7;
            bool ok = (s * BK + ch * 8) < K;
            rb[slot][i] = ok ? *reinterpret_cast<const uint4*>(bg[i] + s * BK) : make_uint4(0, 0, 0, 0);
        }
    };
    f32x4 acc[2][2];
#pragma unroll
    for (int i = 0; i < 2; ++i) { acc[i][0] = (f32x4){0, 0, 0, 0}; acc[i][1] = (f32x4){0, 0, 0, 0}; }
#pragma unroll
    for (int d = 0; d < PF; ++d) gload(d, d);
    int fr = lane & 15, fq = lane >> 4;
#pragma unroll
    for (int s = 0; s < NST; ++s) {
        char* buf = ldsbase + (s & 1) * STG;
        const int slot = s % PF;
#pragma unroll
        for (int i = 0; i < ACH; ++i) *reinterpret_cast<uint4*>(buf + aoff[i]) = ra[slot][i];
#pragma unroll
        for (int i = 0; i < BCH; ++i) *reinterpret_cast<uint4*>(buf + boff[i]) = rb[slot][i];
        if (s + PF < NST) gload(slot, s + PF);
        __syncthreads();
#pragma unroll
        for (int kk = 0; kk < 2; ++kk) {
            bf16x8 af[2], bfr[2];
#pragma unroll
            for (int i = 0; i < 2; ++i) {
                int r = wr * 32 + i * 16 + fr;
                af[i] = *reinterpret_cast<const bf16x8*>(buf + r * 128 + (((kk * 4 + fq) ^ (r & 7)) << 4));
            }
#pragma unroll
            for (int j = 0; j < 2; ++j) {
                int r = wc * 32 + j * 16 + fr;
                bfr[j] = *reinterpret_cast<const bf16x8*>(buf + (BM + r) * 128 + (((kk * 4 + fq) ^ (r & 7)) << 4));
            }
#pragma unroll
            for (int i = 0; i < 2; ++i)
#pragma unroll
                for (int j = 0; j < 2; ++j)
                    acc[i][j] = __builtin_amdgcn_mfma_f32_16x16x32_bf16(bfr[j], af[i], acc[i][j], 0, 0, 0);
        }
    }
#pragma unroll
    for (int i = 0; i < 2; ++i)
#pragma unroll
        for (int j = 0; j < 2; ++j) {
            int m = m0 + wr * 32 + i * 16 + fr;
            int n = n0 + wc * 32 + j * 16 + fq * 4;
            if (n < N) {
                u16 o[4];
#pragma unroll
                for (int r = 0; r < 4; ++r) o[r] = f2bf(silu_f(rbf(acc[i][j][r])));
                uint2 pk = make_uint2((uint32_t)o[0] | ((uint32_t)o[1] << 16), (uint32_t)o[2] | ((uint32_t)o[3] << 16));
                *reinterpret_cast<uint2*>(C + (size_t)m * N + n) = pk;
            }
        }
}

template <int K, int N>
__global__ __launch_bounds__(256) void gemm_nt_silu(const u16* __restrict__ A, const u16* __restrict__ Bt,
                                                     u16* __restrict__ C) {
    __shared__ __attribute__((aligned(16))) char lds[2 * 128 * 128];
    int bid = blockIdx.x;
    int per = gridDim.x / 8;
    bid = (bid % 8) * per + bid / 8;
    gemm_tile_silu<K, N>(A, Bt, C, bid, lds);
}

__global__ __launch_bounds__(256) void k2_colfft_gemm1(float2* __restrict__ XT, const uint32_t* __restrict__ WT,
                                                        const u16* __restrict__ xn, const u16* __restrict__ w1T,
                                                        u16* __restrict__ h) {
    __shared__ __attribute__((aligned(16))) float2 sm[2 * (2048 + 128) + 2048];
    if (blockIdx.x < 256) {
        colfft_body(XT, WT, blockIdx.x, sm, sm + 2176, sm + 2 * 2176);
    } else {
        int bid = blockIdx.x - 256;
        bid = (bid % 8) * 88 + bid / 8;
        gemm_tile_silu<512, 1376>(xn, w1T, h, bid, reinterpret_cast<char*>(sm));
    }
}

__global__ __launch_bounds__(256) void k3_rowc2r(const float2* __restrict__ XT, const u16* __restrict__ x,
                                                  const u16* __restrict__ g, u16* __restrict__ out) {
    __shared__ float2 smem_tw[512];
    __shared__ float2 smem_ex[16 * 272];
    __shared__ float2 smem_st[16 * 257 + 8];
    const int t = threadIdx.x;
    const int s0 = blockIdx.x * 16;
    {
        float2 tmp[16];
#pragma unroll
        for (int it = 0; it < 16; ++it) {
            int i = t + it * 256;
            tmp[it] = XT[(size_t)(i >> 4) * XP + s0 + (i & 15)];
        }
        for (int k = t; k < 512; k += 256) {
            float sn, cs;
            sincospif(-(float)k / 256.0f, &sn, &cs);
            smem_tw[k] = make_float2(cs, sn);
        }
#pragma unroll
        for (int it = 0; it < 16; ++it) {
            int i = t + it * 256;
            smem_st[(i & 15) * 257 + (i >> 4)] = tmp[it];
        }
    }
    const int rl = t >> 4, b = t & 15;
    const int row = s0 + rl;
    const uint32_t* xr = reinterpret_cast<const uint32_t*>(x + (size_t)row * D_LEN);
    const uint32_t* gr = reinterpret_cast<const uint32_t*>(g + (size_t)row * D_LEN);
    uint32_t xp[16], gp[16];
#pragma unroll
    for (int d = 0; d < 16; ++d) { xp[d] = xr[b + 16 * d]; gp[d] = gr[b + 16 * d]; }
    __syncthreads();
    float2 v[16];
    const float2* z = &smem_st[rl * 257];
#pragma unroll
    for (int a = 0; a < 16; ++a) {
        int k = 16 * a + b;
        float2 xk = z[k], xm = z[(256 - k) & 255];
        if (k == 0) { xm = make_float2(xk.y, 0.f); xk = make_float2(xk.x, 0.f); }
        float2 e = make_float2(xk.x + xm.x, xk.y - xm.y);
        float2 o = make_float2(xk.x - xm.x, xk.y + xm.y);
        float2 w = smem_tw[k];
        w.y = -w.y;
        float2 wo = cmul(w, o);
        v[a] = make_float2(e.x - wo.y, e.y + wo.x);
    }
    fft16<1>(v);
#pragma unroll
    for (int c = 1; c < 16; ++c) { float2 w = smem_tw[2 * b * c]; w.y = -w.y; v[c] = cmul(v[c], w); }
    float2* ex = &smem_ex[rl * 272];
#pragma unroll
    for (int c = 0; c < 16; ++c) ex[c * 17 + b] = v[c];
    __builtin_amdgcn_wave_barrier();
    __builtin_amdgcn_fence(__ATOMIC_SEQ_CST, "wavefront");
#pragma unroll
    for (int bb = 0; bb < 16; ++bb) v[bb] = ex[b * 17 + bb];
    fft16<1>(v);
    const float scale = 1.0f / ((float)S_LEN * (float)D_LEN);
    uint32_t* orow = reinterpret_cast<uint32_t*>(out + (size_t)row * D_LEN);
#pragma unroll
    for (int d = 0; d < 16; ++d) {
        float s0v = rbf(v[d].x * scale), s1v = rbf(v[d].y * scale);
        float o0 = bf2f((u16)(xp[d] & 0xffff)) + rbf(s0v * bf2f((u16)(gp[d] & 0xffff)));
        float o1 = bf2f((u16)(xp[d] >> 16)) + rbf(s1v * bf2f((u16)(gp[d] >> 16)));
        orow[b + 16 * d] = (uint32_t)f2bf(o0) | ((uint32_t)f2bf(o1) << 16);
    }
}

extern "C" void kernel_launch(void* const* d_in, const int* in_sizes, int n_in, void* d_out, int out_size, void* d_ws,
                              size_t ws_size, hipStream_t stream) {
    const u16* x = (const u16*)d_in[0];
    const u16* w_r = (const u16*)d_in[1];
    const u16* w_i = (const u16*)d_in[2];
    const u16* w1 = (const u16*)d_in[4];
    const u16* w2 = (const u16*)d_in[5];
    u16* out = (u16*)d_out;
    char* ws = (char*)d_ws;
    u16* xn = (u16*)(ws);
    float2* XT = (float2*)(ws + (2u << 20));
    u16* w1T = (u16*)(ws + (7u << 20));
    u16* w2T = (u16*)(ws + (9u << 20));
    uint32_t* WT = (uint32_t*)(ws + (11u << 20));
    u16* h = (u16*)(ws + (14u << 20));
    u16* g = (u16*)(ws + (20u << 20));

    k1_ln_rowfft<<<K1_ROWBLK + K1_W1BLK + K1_W2BLK + K1_WBLK, 256, 0, stream>>>(x, w_r, w_i, w1, w2, xn, XT, w1T, w2T, WT);
    gemm_nt_silu<512, 1376><<<32 * 22, 256, 0, stream>>>(xn, w1T, h);
    k2_colfft<<<256, 256, 0, stream>>>(XT, WT);
    gemm_nt_silu<1376, 512><<<32 * 8, 256, 0, stream>>>(h, w2T, g);
    k3_rowc2r<<<128, 256, 0, stream>>>(XT, x, g, out);
}
```
